```python
import torch

def _softmax(x, axis=-1):
    return torch.softmax(x, dim=axis)
def _gelu(x):
    return torch.nn.functional.gelu(x, approximate="tanh")
def _silu(x):
    return torch.nn.functional.silu(x)
def _sigmoid(x):
    return torch.sigmoid(x)
def _layernorm(x, axis=-1, eps=1e-5):
    m = x.mean(dim=axis, keepdim=True)
    v = x.var(dim=axis, keepdim=True, unbiased=False)
    return (x - m) / torch.sqrt(v + eps)
def _scatter_add(tgt, idx, src, axis=0):
    return tgt.clone().index_add_(axis, idx, src)
def _segment_sum(src, seg, n):
    out = torch.zeros((n,) + src.shape[1:], dtype=src.dtype)
    return out.index_add_(0, seg, src)
def _pad(x, pw):
    flat = tuple(v for pair in reversed(pw) for v in pair)
    return torch.nn.functional.pad(x, flat)

kernel_name = 'parallel_sum__mha_block_cached__geglu_ffn__decode'

def setup_inputs():

    _w = (lambda a: (lambda t: t.to(torch.bfloat16) if t.is_floating_point() else t)(torch.as_tensor(a)))
    def _A_setup():
        B, Sq, D = 2, 1, 576
        H, Sk, Dh = 32, 8192, 128
        return {
            "x": _w((lambda *s: torch.randn(*s, dtype=torch.bfloat16) * 0.05)(B, Sq, D)),
            "k_cache": _w((lambda *s: torch.randn(*s, dtype=torch.bfloat16) * 3.2)(B, H, Sk, Dh)),
            "v_cache": _w((lambda *s: torch.randn(*s, dtype=torch.bfloat16) * 1.6)(B, H, Sk, Dh)),
            "wq": _w((lambda *s: torch.randn(*s, dtype=torch.bfloat16) * 3.2)(D, H * Dh) * (D ** -0.5)),
            "wo": _w((lambda *s: torch.randn(*s, dtype=torch.bfloat16) * 1.6)(H * Dh, D) * ((H * Dh) ** -0.5)),
        }
    _A_in = _A_setup()

    def _B_setup():
        B, S, D, F = 2, 1, 576, 4608
        return {
            "x": _w((lambda *s: torch.randn(*s, dtype=torch.bfloat16) * 0.05)(B, S, D)),
            "w_gate": _w((lambda *s: torch.randn(*s, dtype=torch.bfloat16) * 0.05)(D, F)),
            "w_up": _w((lambda *s: torch.randn(*s, dtype=torch.bfloat16) * 0.05)(D, F)),
            "w_down": _w((lambda *s: torch.randn(*s, dtype=torch.bfloat16) * 0.05)(F, D)),
        }
    _B_in = _B_setup()

    return {
        "A_x": _A_in["x"],
        "A_k_cache": _A_in["k_cache"],
        "A_v_cache": _A_in["v_cache"],
        "A_wq": _A_in["wq"],
        "A_wo": _A_in["wo"],
        "B_x": _B_in["x"],
        "B_w_gate": _B_in["w_gate"],
        "B_w_up": _B_in["w_up"],
        "B_w_down": _B_in["w_down"],
    }

def reference(A_x, A_k_cache, A_v_cache, A_wq, A_wo, B_x, B_w_gate, B_w_up, B_w_down):
    def _A_ref(x, k_cache, v_cache, wq, wo):
        B, Sq, D = x.shape
        H, Dh = 32, 128
        q = (lambda x, *d: x.permute(*d))((x @ wq).reshape(B, Sq, H, Dh), 0, 2, 1, 3)
        scores = torch.einsum("bhqd,bhkd->bhqk", q, k_cache) * (Dh ** -0.5)
        p = _softmax(scores, axis=-1)
        o = torch.einsum("bhqk,bhkd->bhqd", p, v_cache)
        return (lambda x, *d: x.permute(*d))(o, 0, 2, 1, 3).reshape(B, Sq, H * Dh) @ wo

    def _B_ref(x, w_gate, w_up, w_down):
        return (_gelu(x @ w_gate) * (x @ w_up)) @ w_down

    x = A_x
    xn = _layernorm(x, axis=-1)
    return x + _A_ref(xn, A_k_cache, A_v_cache, A_wq, A_wo) + _B_ref(xn, B_w_gate, B_w_up, B_w_down)
```

```cpp
#include <hip/hip_runtime.h>
#include <hip/hip_bf16.h>
#include <stdint.h>

#define MIMIC 0

typedef unsigned int u32;
typedef unsigned short u16;
using u32x4 = __attribute__((ext_vector_type(4))) unsigned;
using bf16x2 = __attribute__((ext_vector_type(2))) __bf16;

constexpr int NB = 2, DM = 576, NH = 32, SK = 8192, DH = 128, FF = 4608, HD = NH * DH;
constexpr int NSPLIT = 4;

__device__ __forceinline__ float bf2f(u16 b) { return __builtin_bit_cast(float, (u32)b << 16); }
__device__ __forceinline__ float rbf(float f) {
  return __bfloat162float(__float2bfloat16(f));
}
__device__ __forceinline__ float mrbf(float f) { return MIMIC ? rbf(f) : f; }
__device__ __forceinline__ float lo(u32 v) { return __builtin_bit_cast(float, v << 16); }
__device__ __forceinline__ float hi(u32 v) { return __builtin_bit_cast(float, v & 0xffff0000u); }

__device__ void layernorm_to_lds(const u16* x, float* xn  , float* red) {
  int tid = threadIdx.x, lane = tid & 63, w = tid >> 6;
  if (w < 2) {
    float v[9]; float s = 0.f;
    for (int i = 0; i < 9; ++i) { v[i] = bf2f(x[w * DM + i * 64 + lane]); s += v[i]; }
    for (int o = 32; o; o >>= 1) s += __shfl_xor(s, o);
    float mean = s * (1.0f / DM);
    float q = 0.f;
    for (int i = 0; i < 9; ++i) { float d = v[i] - mean; q += d * d; }
    for (int o = 32; o; o >>= 1) q += __shfl_xor(q, o);
    float var = q * (1.0f / DM);
    float mb = mrbf(mean), vb = mrbf(var);
    float den = mrbf(sqrtf(mrbf(vb + 1e-5f)));
    for (int i = 0; i < 9; ++i) xn[w * DM + i * 64 + lane] = mrbf(mrbf(v[i] - mb) / den);
  }
  __syncthreads();
}

__device__ __forceinline__ float gelu_tanh(float x) {
  const float k0 = 0.7978845608028654f, k1 = 0.044715f;
  float inner = k0 * (x + k1 * x * x * x);
  return 0.5f * x * (1.0f + tanhf(inner));
}

__global__ void __launch_bounds__(256) k1(const u16* x, const u16* wq, const u16* wg, const u16* wu, float* q, float* h) {
  __shared__ float xn[2 * DM];
  __shared__ float red[8][32][8];
  layernorm_to_lds(x, xn, nullptr);
  int tid = threadIdx.x, cp = tid & 31, rg = tid >> 5;
  int blk = blockIdx.x;
  if (blk < 64) {
    int col = blk * 64 + cp * 2;
    float a00 = 0, a01 = 0, a10 = 0, a11 = 0;
    for (int k = rg; k < DM; k += 8) {
      u32 w = *(const u32*)(wq + (size_t)k * HD + col);
      float x0 = xn[k], x1 = xn[DM + k];
      a00 += x0 * lo(w); a01 += x0 * hi(w); a10 += x1 * lo(w); a11 += x1 * hi(w);
    }
    red[rg][cp][0] = a00; red[rg][cp][1] = a01; red[rg][cp][2] = a10; red[rg][cp][3] = a11;
    __syncthreads();
    if (tid < 128) {
      int c = tid >> 2, j = tid & 3; float s = 0;
      for (int r = 0; r < 8; ++r) s += red[r][c][j];
      q[(j >> 1) * HD + blk * 64 + c * 2 + (j & 1)] = mrbf(s);
    }
  } else {
    int cb = blk - 64;
    int col = cb * 64 + cp * 2;
    float a[8] = {0, 0, 0, 0, 0, 0, 0, 0};
    for (int k = rg; k < DM; k += 8) {
      u32 g = *(const u32*)(wg + (size_t)k * FF + col);
      u32 u = *(const u32*)(wu + (size_t)k * FF + col);
      float x0 = xn[k], x1 = xn[DM + k];
      a[0] += x0 * lo(g); a[1] += x0 * hi(g); a[2] += x1 * lo(g); a[3] += x1 * hi(g);
      a[4] += x0 * lo(u); a[5] += x0 * hi(u); a[6] += x1 * lo(u); a[7] += x1 * hi(u);
    }
    for (int j = 0; j < 8; ++j) red[rg][cp][j] = a[j];
    __syncthreads();
    if (tid < 128) {
      int c = tid >> 2, j = tid & 3; float sg = 0, su = 0;
      for (int r = 0; r < 8; ++r) { sg += red[r][c][j]; su += red[r][c][4 + j]; }
      float g = mrbf(gelu_tanh(mrbf(sg)));
      h[(j >> 1) * FF + cb * 64 + c * 2 + (j & 1)] = mrbf(g * mrbf(su));
    }
  }
}

template <int CTRL> __device__ __forceinline__ float dpp(float x) {
  return __builtin_bit_cast(float, __builtin_amdgcn_mov_dpp(__builtin_bit_cast(int, x), CTRL, 0xf, 0xf, true));
}
constexpr int XOR1 = 0xB1, XOR2 = 0x4E, XOR7 = 0x141, XOR8 = 0x128;

__global__ void __launch_bounds__(512) k2(const float* q, const u16* kc, const u16* vc, float* pml, float* po) {
  __shared__ float sm[8], sl[8], sacc[8][128];
  int tid = threadIdx.x, lane = tid & 63, w = tid >> 6, r = lane >> 4, c = lane & 15;
  int bh = blockIdx.x / NSPLIT, s = blockIdx.x % NSPLIT;
  int b = bh / NH, h = bh % NH;
  float qf[8];
  for (int j = 0; j < 8; ++j) qf[j] = q[b * HD + h * DH + c * 8 + j];
  const u16* kb = kc + ((size_t)bh * SK + s * (SK / NSPLIT)) * DH;
  const u16* vb = vc + ((size_t)bh * SK + s * (SK / NSPLIT)) * DH;
  float m = -INFINITY, l = 0.f, acc[8] = {0, 0, 0, 0, 0, 0, 0, 0};
  const float scale = 0.08838834764831845f;
  for (int i = 0; i < 64; ++i) {
    int key = (i * 8 + w) * 4 + r;
    u32x4 kk = __builtin_nontemporal_load((const u32x4*)(kb + (size_t)key * DH + c * 8));
    u32x4 vv = __builtin_nontemporal_load((const u32x4*)(vb + (size_t)key * DH + c * 8));
    float d = 0.f;
    for (int j = 0; j < 4; ++j) { d += qf[2 * j] * lo(kk[j]); d += qf[2 * j + 1] * hi(kk[j]); }
    d += dpp<XOR1>(d); d += dpp<XOR2>(d); d += dpp<XOR7>(d); d += dpp<XOR8>(d);
    float sc = mrbf(mrbf(d) * scale);
    float mn = fmaxf(m, sc);
    float al = __expf(m - mn), p = __expf(sc - mn);
    l = l * al + p; m = mn;
    for (int j = 0; j < 4; ++j) { acc[2 * j] = acc[2 * j] * al + p * lo(vv[j]); acc[2 * j + 1] = acc[2 * j + 1] * al + p * hi(vv[j]); }
  }
  float M = fmaxf(m, __shfl_xor(m, 16)); M = fmaxf(M, __shfl_xor(M, 32));
  float f = __expf(m - M);
  l *= f; l += __shfl_xor(l, 16); l += __shfl_xor(l, 32);
  for (int j = 0; j < 8; ++j) { float a = acc[j] * f; a += __shfl_xor(a, 16); a += __shfl_xor(a, 32); acc[j] = a; }
  if (lane == 0) { sm[w] = M; sl[w] = l; }
  if (r == 0) for (int j = 0; j < 8; ++j) sacc[w][c * 8 + j] = acc[j];
  __syncthreads();
  if (tid < 128) {
    float MM = -INFINITY;
    for (int i = 0; i < 8; ++i) MM = fmaxf(MM, sm[i]);
    float L = 0, o = 0;
    for (int i = 0; i < 8; ++i) { float ww = __expf(sm[i] - MM); L += ww * sl[i]; o += ww * sacc[i][tid]; }
    po[(size_t)blockIdx.x * 128 + tid] = o;
    if (tid == 0) { pml[blockIdx.x * 2] = MM; pml[blockIdx.x * 2 + 1] = L; }
  }
}

__global__ void __launch_bounds__(512) k3(const u16* x, const float* pml, const float* po, const float* h, const u16* wo, const u16* wd, __hip_bfloat16* out) {
  __shared__ float o[2 * HD];
  __shared__ float hh[2 * FF];
  __shared__ float red[512][17];
  int tid = threadIdx.x;
  for (int i = tid; i < 2 * HD; i += 512) {
    int bh = i / DH, d = i % DH;
    float M = -INFINITY;
    for (int s = 0; s < NSPLIT; ++s) M = fmaxf(M, pml[(bh * NSPLIT + s) * 2]);
    float L = 0, a = 0;
    for (int s = 0; s < NSPLIT; ++s) { float ww = __expf(pml[(bh * NSPLIT + s) * 2] - M); L += ww * pml[(bh * NSPLIT + s) * 2 + 1]; a += ww * po[(size_t)(bh * NSPLIT + s) * 128 + d]; }
    o[i] = mrbf(a / L);
  }
  for (int i = tid; i < 2 * FF; i += 512) hh[i] = h[i];
  __syncthreads();
  int col = blockIdx.x * 8;
  float a[16];
  float resA = 0.f;
  for (int pass = 0; pass < 2; ++pass) {
    for (int j = 0; j < 16; ++j) a[j] = 0.f;
    const u16* W = pass ? wd : wo; const float* v = pass ? hh : o; int K = pass ? FF : HD;
    for (int k = tid; k < K; k += 512) {
      u32x4 ww = *(const u32x4*)(W + (size_t)k * DM + col);
      float x0 = v[k], x1 = v[K + k];
      for (int j = 0; j < 4; ++j) { a[2 * j] += x0 * lo(ww[j]); a[2 * j + 1] += x0 * hi(ww[j]); a[8 + 2 * j] += x1 * lo(ww[j]); a[8 + 2 * j + 1] += x1 * hi(ww[j]); }
    }
    for (int j = 0; j < 16; ++j) red[tid][j] = a[j];
    __syncthreads();
    if (tid < 16) {
      float s = 0; for (int i = 0; i < 512; ++i) s += red[i][tid];
      int b = tid >> 3, cc = col + (tid & 7);
      if (pass == 0) resA = mrbf(bf2f(x[b * DM + cc]) + mrbf(s));
      else out[b * DM + cc] = __float2bfloat16(resA + mrbf(s));
    }
    __syncthreads();
  }
}

extern "C" void kernel_launch(void* const* d_in, const int* in_sizes, int n_in, void* d_out, int out_size, void* d_ws, size_t ws_size, hipStream_t stream) {
  const u16* x = (const u16*)d_in[0];
  const u16* kc = (const u16*)d_in[1];
  const u16* vc = (const u16*)d_in[2];
  const u16* wq = (const u16*)d_in[3];
  const u16* wo = (const u16*)d_in[4];
  const u16* wg = (const u16*)d_in[6];
  const u16* wu = (const u16*)d_in[7];
  const u16* wd = (const u16*)d_in[8];
  float* ws = (float*)d_ws;
  float* q = ws; float* h = ws + 8192; float* pml = ws + 32768; float* po = ws + 65536;
  k1<<<136, 256, 0, stream>>>(x, wq, wg, wu, q, h);
  k2<<<256, 512, 0, stream>>>(q, kc, vc, pml, po);
  k3<<<72, 512, 0, stream>>>(x, pml, po, h, wo, wd, (__hip_bfloat16*)d_out);
}
```

```cpp
#include <hip/hip_runtime.h>
#include <hip/hip_bf16.h>
#include <stdint.h>

#define KV_NT 1
#define W_NT 0
#define STAMPS 0

typedef unsigned int u32;
typedef unsigned short u16;
typedef unsigned long long u64;
using u32x4 = __attribute__((ext_vector_type(4))) unsigned;
using u32x2 = __attribute__((ext_vector_type(2))) unsigned;
using f32x4 = __attribute__((ext_vector_type(4))) float;
using f32x2 = __attribute__((ext_vector_type(2))) float;
using bf16x2 = __attribute__((ext_vector_type(2))) __bf16;
#define GAS __attribute__((address_space(1)))
#define RLX_AGENT __ATOMIC_RELAXED, __HIP_MEMORY_SCOPE_AGENT

constexpr int DM = 576, NH = 32, SK = 8192, DH = 128, FF = 4608, HD = NH * DH;
constexpr int NWAVE = 8, NTHR = 512, W = 16;
constexpr int WS_EPOCH = 0;
constexpr int WS_STAT = 1024;
constexpr int WS_Y = 8192;
constexpr int WS_STAMP = 1 << 20;
constexpr unsigned SPIN_CAP = 1u << 18;

struct Params {
  const u16 *x, *kc, *vc, *wq, *wo, *wg, *wu, *wd;
  u16* out;
  u64* ws;
};

__device__ __forceinline__ float lo16(u32 v) { return __builtin_bit_cast(float, v << 16); }
__device__ __forceinline__ float hi16(u32 v) { return __builtin_bit_cast(float, v & 0xffff0000u); }
__device__ __forceinline__ u32 pkbf(float a, float b) {
  bf16x2 r; r[0] = (__bf16)a; r[1] = (__bf16)b; return __builtin_bit_cast(u32, r);
}
__device__ __forceinline__ float rbf(float f) { return lo16(pkbf(f, f)); }
__device__ __forceinline__ float dot2(u32 k, u32 q, float acc) {
  return __builtin_amdgcn_fdot2_f32_bf16(__builtin_bit_cast(bf16x2, k), __builtin_bit_cast(bf16x2, q), acc, false);
}
template <int CTRL> __device__ __forceinline__ float dpp(float x) {
  return __builtin_bit_cast(float, __builtin_amdgcn_mov_dpp(__builtin_bit_cast(int, x), CTRL, 0xf, 0xf, true));
}
constexpr int XOR1 = 0xB1, XOR2 = 0x4E, XOR7 = 0x141, XOR8 = 0x128;
__device__ __forceinline__ float row_sum16(float d) { d += dpp<XOR1>(d); d += dpp<XOR2>(d); d += dpp<XOR7>(d); d += dpp<XOR8>(d); return d; }
__device__ __forceinline__ float row_max16(float d) { d = fmaxf(d, dpp<XOR1>(d)); d = fmaxf(d, dpp<XOR2>(d)); d = fmaxf(d, dpp<XOR7>(d)); d = fmaxf(d, dpp<XOR8>(d)); return d; }
__device__ __forceinline__ float x16_sum(float x) {
  auto s = __builtin_amdgcn_permlane16_swap(__float_as_uint(x), __float_as_uint(x), false, false);
  return __uint_as_float(s[0]) + __uint_as_float(s[1]);
}
__device__ __forceinline__ float x32_sum(float x) {
  auto t = __builtin_amdgcn_permlane32_swap(__float_as_uint(x), __float_as_uint(x), false, false);
  return __uint_as_float(t[0]) + __uint_as_float(t[1]);
}
__device__ __forceinline__ float x16_max(float x) {
  auto s = __builtin_amdgcn_permlane16_swap(__float_as_uint(x), __float_as_uint(x), false, false);
  return fmaxf(__uint_as_float(s[0]), __uint_as_float(s[1]));
}
__device__ __forceinline__ float x32_max(float x) {
  auto t = __builtin_amdgcn_permlane32_swap(__float_as_uint(x), __float_as_uint(x), false, false);
  return fmaxf(__uint_as_float(t[0]), __uint_as_float(t[1]));
}
__device__ __forceinline__ float xrow_sum(float x) { return x32_sum(x16_sum(x)); }
__device__ __forceinline__ float wave_sum(float x) { return xrow_sum(row_sum16(x)); }
__device__ __forceinline__ float wave_max(float x) { return x32_max(x16_max(row_max16(x))); }
__device__ __forceinline__ float rdlane(float x, int l) { return __builtin_bit_cast(float, __builtin_amdgcn_readlane(__builtin_bit_cast(int, x), l)); }

__device__ __forceinline__ u32x4 ldkv(const GAS u32x4* p) { return KV_NT ? __builtin_nontemporal_load(p) : *p; }
__device__ __forceinline__ u32x4 ldw4(const GAS u32x4* p) { return W_NT ? __builtin_nontemporal_load(p) : *p; }
__device__ __forceinline__ u32x2 ldw2(const GAS u32x2* p) { return W_NT ? __builtin_nontemporal_load(p) : *p; }
__device__ __forceinline__ void st_gran(GAS u64* g, u32 tag, float v) {
  __hip_atomic_store(g, ((u64)tag << 32) | (u64)__builtin_bit_cast(u32, v), RLX_AGENT);
}
__device__ __forceinline__ u64 ld_gran(GAS u64* g) { return __hip_atomic_load(g, RLX_AGENT); }
#define SB() __builtin_amdgcn_sched_barrier(0)
#define STAMP(i)

__device__ __forceinline__ void fma8(float (&a)[8], float s, u32x4 v) {
#pragma unroll
  for (int k = 0; k < 4; ++k) { a[2 * k] = fmaf(s, lo16(v[k]), a[2 * k]); a[2 * k + 1] = fmaf(s, hi16(v[k]), a[2 * k + 1]); }
}

__device__ __forceinline__ void pin8(float (&a)[8]) {
  asm volatile("" : "+v"(a[0]), "+v"(a[1]), "+v"(a[2]), "+v"(a[3]), "+v"(a[4]), "+v"(a[5]), "+v"(a[6]), "+v"(a[7]));
}
template <int J> struct KStep {
  static __device__ __forceinline__ void run(u32x4 (&ring)[W], const u32 (&q)[4], float& keep, int c, const GAS u32x4* nx) {
    float d = dot2(ring[J][0], q[0], 0.f);
    d = dot2(ring[J][1], q[1], d); d = dot2(ring[J][2], q[2], d); d = dot2(ring[J][3], q[3], d);
    SB();
    ring[J] = ldkv(nx + J * 512);
    d = row_sum16(d);
    keep = (c == J) ? d : keep;
    SB();
    if constexpr (J + 1 < W) KStep<J + 1>::run(ring, q, keep, c, nx);
  }
};
template <int J, bool REFILL> struct VStep {
  static __device__ __forceinline__ void run(u32x4 (&ring)[W], float pc, float (&acc)[8], const GAS u32x4* nx) {
    const float pj = dpp<0x150 + J>(pc);
    fma8(acc, pj, ring[J]);
    pin8(acc);
    if constexpr (REFILL) { SB(); ring[J] = ldkv(nx + J * 512); SB(); }
    if constexpr (J + 1 < W) VStep<J + 1, REFILL>::run(ring, pc, acc, nx);
  }
};

__global__ void __launch_bounds__(NTHR, 2) fused_decode(Params p) {
  __shared__ __attribute__((aligned(16))) float lds[576 + 8 * 128 + 8 * 72 + 8 * 128 + 8 * 576];
  float* xn = lds;
  float* qred = lds + 576;
  float* fred = qred + 8 * 128;
  float* ored = fred + 8 * 72;
  float* yred = ored + 8 * 128;
  const int tid = threadIdx.x, lane = tid & 63, w = __builtin_amdgcn_readfirstlane(tid >> 6), r = lane >> 4, c = lane & 15;
  const int bid = blockIdx.x, xg = bid & 7, j = bid >> 3;
  const int h = xg * 4 + (j >> 3), b = (j >> 2) & 1, s = j & 3;
  const int pi = h * 4 + s;
  const int bh = b * NH + h;
  GAS u64* ws = (GAS u64*)p.ws;
  STAMP(0);

  const GAS u32x4* xp = (const GAS u32x4*)(p.x + b * DM);
  const u32x4 xa = xp[lane];
  const u32x4 xb = xp[64 + (lane & 7)];
  const u32 tag = (u32)__hip_atomic_load((GAS u32*)(ws + WS_EPOCH + b * 32), RLX_AGENT) + 1u;
  const u16 xres = ((const GAS u16*)p.x)[b * DM + (pi < 72 ? pi : 0) * 8 + (lane & 7)];
  u32x4 side[18];
  {
    const GAS u32x4* wqp = (const GAS u32x4*)(p.wq + (size_t)(w * 72 + r) * HD + h * DH + c * 8);
#pragma unroll
    for (int i = 0; i < 18; ++i) side[i] = ldw4(wqp + (size_t)i * (4 * HD / 8));
  }
  SB();
  const size_t kvoff = ((size_t)bh * SK + s * 2048) * (DH / 8) + w * 64 + lane;
  const GAS u32x4* kp = (const GAS u32x4*)p.kc + kvoff;
  const GAS u32x4* vp = (const GAS u32x4*)p.vc + kvoff;
  u32x4 ring[W];
#pragma unroll
  for (int i = 0; i < W; ++i) ring[i] = ldkv(kp + i * 512);
  SB();

  {
    float v[16];
#pragma unroll
    for (int k = 0; k < 4; ++k) { v[2 * k] = lo16(xa[k]); v[2 * k + 1] = hi16(xa[k]); v[8 + 2 * k] = lo16(xb[k]); v[8 + 2 * k + 1] = hi16(xb[k]); }
    float s0 = 0.f, s1 = 0.f;
#pragma unroll
    for (int k = 0; k < 8; ++k) { s0 += v[k]; s1 += v[8 + k]; }
    const bool t8 = lane < 8;
    const float mean = wave_sum(s0 + (t8 ? s1 : 0.f)) * (1.0f / DM);
    float q0 = 0.f, q1 = 0.f;
#pragma unroll
    for (int k = 0; k < 8; ++k) { float d0 = v[k] - mean, d1 = v[8 + k] - mean; q0 = fmaf(d0, d0, q0); q1 = fmaf(d1, d1, q1); }
    const float var = wave_sum(q0 + (t8 ? q1 : 0.f)) * (1.0f / DM);
    const float mb = rbf(mean), den = rbf(sqrtf(rbf(rbf(var) + 1e-5f)));
    float o[16];
#pragma unroll
    for (int k = 0; k < 16; ++k) o[k] = rbf(rbf(v[k] - mb) / den);
    *(f32x4*)(xn + lane * 8) = f32x4{o[0], o[1], o[2], o[3]};
    *(f32x4*)(xn + lane * 8 + 4) = f32x4{o[4], o[5], o[6], o[7]};
    *(f32x4*)(xn + 512 + (lane & 7) * 8) = f32x4{o[8], o[9], o[10], o[11]};
    *(f32x4*)(xn + 512 + (lane & 7) * 8 + 4) = f32x4{o[12], o[13], o[14], o[15]};
  }
  STAMP(1);
  u32 q[4];
  {
    float a[8] = {0, 0, 0, 0, 0, 0, 0, 0};
#pragma unroll
    for (int i = 0; i < 18; ++i) fma8(a, xn[w * 72 + 4 * i + r], side[i]);
#pragma unroll
    for (int k = 0; k < 8; ++k) a[k] = xrow_sum(a[k]);
    if (r == 0) { *(f32x4*)(qred + w * 128 + c * 8) = f32x4{a[0], a[1], a[2], a[3]}; *(f32x4*)(qred + w * 128 + c * 8 + 4) = f32x4{a[4], a[5], a[6], a[7]}; }
    __syncthreads();
    f32x4 t0 = {0, 0, 0, 0}, t1 = {0, 0, 0, 0};
#pragma unroll
    for (int ww = 0; ww < 8; ++ww) { t0 += *(const f32x4*)(qred + ww * 128 + c * 8); t1 += *(const f32x4*)(qred + ww * 128 + c * 8 + 4); }
    q[0] = pkbf(t0[0], t0[1]); q[1] = pkbf(t0[2], t0[3]); q[2] = pkbf(t1[0], t1[1]); q[3] = pkbf(t1[2], t1[3]);
  }
  STAMP(2);
  u32x2 gw[18], uw[18];
  u32x4 dw[6];
  {
    const int cc = c < 9 ? c : 8;
    const size_t go = (size_t)(w * 72 + r) * FF + pi * 36 + cc * 4;
    const GAS u32x2* gp = (const GAS u32x2*)(p.wg + go);
    const GAS u32x2* up = (const GAS u32x2*)(p.wu + go);
#pragma unroll
    for (int i = 0; i < 18; ++i) { gw[i] = ldw2(gp + (size_t)i * (4 * FF / 4)); uw[i] = ldw2(up + (size_t)i * (4 * FF / 4)); }
    const GAS u32x4* dp = (const GAS u32x4*)(p.wd + (size_t)pi * 36 * DM);
#pragma unroll
    for (int i = 0; i < 5; ++i) { const int row = (w + 8 * i < 36) ? w + 8 * i : w; dw[i] = ldw4(dp + row * 72 + lane); }
    { const int row = (w + 8 * (lane >> 3) < 36) ? w + 8 * (lane >> 3) : w; dw[5] = ldw4(dp + row * 72 + 64 + (lane & 7)); }
  }
  SB();
  float sc0 = 0.f, sc1 = 0.f, sc2 = 0.f, sc3 = 0.f;
  const float SCALE = 0.08838834764831845f;
#pragma nounroll
  for (int it = 0; it < 2; ++it) {
    float keep = 0.f;
    KStep<0>::run(ring, q, keep, c, kp + (it + 1) * (W * 512));
    sc0 = sc1; sc1 = rbf(rbf(keep) * SCALE);
  }
  STAMP(3);
  float ym[8], yt[8];
  {
    float g[4] = {0, 0, 0, 0}, u[4] = {0, 0, 0, 0};
#pragma unroll
    for (int i = 0; i < 18; ++i) {
      const float xv = xn[w * 72 + 4 * i + r];
      g[0] = fmaf(xv, lo16(gw[i][0]), g[0]); g[1] = fmaf(xv, hi16(gw[i][0]), g[1]); g[2] = fmaf(xv, lo16(gw[i][1]), g[2]); g[3] = fmaf(xv, hi16(gw[i][1]), g[3]);
      u[0] = fmaf(xv, lo16(uw[i][0]), u[0]); u[1] = fmaf(xv, hi16(uw[i][0]), u[1]); u[2] = fmaf(xv, lo16(uw[i][1]), u[2]); u[3] = fmaf(xv, hi16(uw[i][1]), u[3]);
    }
    SB();
    const GAS u32x4* wop = (const GAS u32x4*)(p.wo + (size_t)(h * DH + w * 16) * DM);
#pragma unroll
    for (int i = 0; i < 16; ++i) side[i] = ldw4(wop + i * 72 + lane);
    side[16] = ldw4(wop + (lane >> 3) * 72 + 64 + (lane & 7));
    side[17] = ldw4(wop + (8 + (lane >> 3)) * 72 + 64 + (lane & 7));
    SB();
#pragma unroll
    for (int k = 0; k < 4; ++k) { g[k] = xrow_sum(g[k]); u[k] = xrow_sum(u[k]); }
    if (lane < 9) { *(f32x4*)(fred + w * 72 + lane * 4) = f32x4{g[0], g[1], g[2], g[3]}; *(f32x4*)(fred + w * 72 + 36 + lane * 4) = f32x4{u[0], u[1], u[2], u[3]}; }
    __syncthreads();
    const int hr = w + 8 * (lane >> 3);
    const bool hv = hr < 36;
    const int hc = hv ? hr : 0;
    float gs = fred[(lane & 7) * 72 + hc], us = fred[(lane & 7) * 72 + 36 + hc];
    gs += dpp<XOR1>(gs); gs += dpp<XOR2>(gs); gs += dpp<XOR7>(gs);
    us += dpp<XOR1>(us); us += dpp<XOR2>(us); us += dpp<XOR7>(us);
    gs = rbf(gs); us = rbf(us);
    const float inner = 0.7978845608028654f * (gs + 0.044715f * gs * gs * gs);
    const float ge = rbf(gs / (1.0f + __expf(-2.0f * inner)));
    const float hh = hv ? rbf(ge * us) : 0.f;
#pragma unroll
    for (int k = 0; k < 8; ++k) { ym[k] = 0.f; yt[k] = 0.f; }
#pragma unroll
    for (int i = 0; i < 5; ++i) fma8(ym, rdlane(hh, 8 * i), dw[i]);
    fma8(yt, hh, dw[5]);
    pin8(ym); pin8(yt);
  }
  STAMP(4);
#pragma nounroll
  for (int it = 2; it < 4; ++it) {
    float keep = 0.f;
    KStep<0>::run(ring, q, keep, c, it == 3 ? vp : kp + 3 * (W * 512));
    sc2 = sc3; sc3 = rbf(rbf(keep) * SCALE);
  }
  STAMP(5);
  const float LOG2E = 1.4426950408889634f;
  const float mw = wave_max(fmaxf(fmaxf(sc0, sc1), fmaxf(sc2, sc3)));
  float p0 = __builtin_amdgcn_exp2f((sc0 - mw) * LOG2E), p1 = __builtin_amdgcn_exp2f((sc1 - mw) * LOG2E);
  float p2 = __builtin_amdgcn_exp2f((sc2 - mw) * LOG2E), p3 = __builtin_amdgcn_exp2f((sc3 - mw) * LOG2E);
  {
    const float lw = wave_sum((p0 + p1) + (p2 + p3));
    if (lane < 2) st_gran(ws + WS_STAT + bh * 64 + lane * 32 + s * 8 + w, tag, lane ? lw : mw);
  }
  SB();
  float acc[8] = {0, 0, 0, 0, 0, 0, 0, 0};
#pragma nounroll
  for (int it = 0; it < 3; ++it) {
    VStep<0, true>::run(ring, p0, acc, vp + (it + 1) * (W * 512));
    p0 = p1; p1 = p2; p2 = p3;
  }
  STAMP(6);
  u64 sg = ld_gran(ws + WS_STAT + bh * 64 + lane);
  SB();
  VStep<0, false>::run(ring, p0, acc, vp);
  STAMP(7);
  for (unsigned spins = 0; !__all((u32)(sg >> 32) == tag) && spins < SPIN_CAP; ++spins) {
    __builtin_amdgcn_s_sleep(2);
    sg = ld_gran(ws + WS_STAT + bh * 64 + lane);
  }
  {
    const u32 sv = (u32)sg;
    auto t = __builtin_amdgcn_permlane32_swap(sv, sv, false, false);
    const float mi = __uint_as_float(t[0]), li = __uint_as_float(t[1]);
    const float M = x16_max(row_max16(mi));
    const float L = x16_sum(row_sum16(li * __builtin_amdgcn_exp2f((mi - M) * LOG2E)));
    const float f = __builtin_amdgcn_exp2f((mw - M) * LOG2E) / L;
#pragma unroll
    for (int k = 0; k < 8; ++k) acc[k] = xrow_sum(acc[k]) * f;
    if (r == 0) { *(f32x4*)(ored + w * 128 + c * 8) = f32x4{acc[0], acc[1], acc[2], acc[3]}; *(f32x4*)(ored + w * 128 + c * 8 + 4) = f32x4{acc[4], acc[5], acc[6], acc[7]}; }
  }
  __syncthreads();
  STAMP(8);
  {
    float oa = ored[(lane & 7) * 128 + 16 * w + (lane >> 3)], ob = ored[(lane & 7) * 128 + 16 * w + 8 + (lane >> 3)];
    oa += dpp<XOR1>(oa); oa += dpp<XOR2>(oa); oa += dpp<XOR7>(oa);
    ob += dpp<XOR1>(ob); ob += dpp<XOR2>(ob); ob += dpp<XOR7>(ob);
#pragma unroll
    for (int i = 0; i < 8; ++i) { fma8(ym, rdlane(oa, 8 * i), side[i]); fma8(ym, rdlane(ob, 8 * i), side[8 + i]); }
    fma8(yt, oa, side[16]); fma8(yt, ob, side[17]);
#pragma unroll
    for (int k = 0; k < 8; ++k) { float t = yt[k]; t += dpp<XOR8>(t); yt[k] = xrow_sum(t); }
    *(f32x4*)(yred + w * 576 + lane * 8) = f32x4{ym[0], ym[1], ym[2], ym[3]};
    *(f32x4*)(yred + w * 576 + lane * 8 + 4) = f32x4{ym[4], ym[5], ym[6], ym[7]};
    if (lane < 8) { *(f32x4*)(yred + w * 576 + 512 + lane * 8) = f32x4{yt[0], yt[1], yt[2], yt[3]}; *(f32x4*)(yred + w * 576 + 512 + lane * 8 + 4) = f32x4{yt[4], yt[5], yt[6], yt[7]}; }
  }
  __syncthreads();
  STAMP(9);
  GAS u64* yg = ws + WS_Y + (size_t)b * (72 * 1024);
  {
    float t = 0.f;
#pragma unroll
    for (int ww = 0; ww < 8; ++ww) t += yred[ww * 576 + tid];
    st_gran(yg + (tid >> 3) * 1024 + pi * 8 + (tid & 7), tag, t);
    if (w == 7) {
      float t2 = 0.f;
#pragma unroll
      for (int ww = 0; ww < 8; ++ww) t2 += yred[ww * 576 + 512 + lane];
      st_gran(yg + (64 + (lane >> 3)) * 1024 + pi * 8 + (lane & 7), tag, t2);
    }
  }
  STAMP(10);
  if (w == 0 && pi < 72) {
    GAS u64* g = yg + pi * 1024 + lane;
    u64 v[16];
#pragma unroll
    for (int k = 0; k < 16; ++k) v[k] = ld_gran(g + k * 64);
    for (unsigned spins = 0; spins < SPIN_CAP; ++spins) {
      bool ok = true;
#pragma unroll
      for (int k = 0; k < 16; ++k) ok &= (u32)(v[k] >> 32) == tag;
      if (__all(ok)) break;
      __builtin_amdgcn_s_sleep(4);
#pragma unroll
      for (int k = 0; k < 16; ++k) if ((u32)(v[k] >> 32) != tag) v[k] = ld_gran(g + k * 64);
    }
    float t = 0.f;
#pragma unroll
    for (int k = 0; k < 16; ++k) t += __uint_as_float((u32)v[k]);
    t += dpp<XOR8>(t);
    t = xrow_sum(t);
    const float res = __builtin_bit_cast(float, (u32)xres << 16) + t;
    if (lane < 8) ((GAS u16*)p.out)[b * DM + pi * 8 + lane] = (u16)(pkbf(res, res) & 0xffffu);
    if (pi == 0 && lane == 0) __hip_atomic_store((GAS u32*)(ws + WS_EPOCH + b * 32), tag, RLX_AGENT);
  }
  STAMP(11);
}

extern "C" void kernel_launch(void* const* d_in, const int* in_sizes, int n_in, void* d_out, int out_size, void* d_ws, size_t ws_size, hipStream_t stream) {
  Params p;
  p.x = (const u16*)d_in[0]; p.kc = (const u16*)d_in[1]; p.vc = (const u16*)d_in[2]; p.wq = (const u16*)d_in[3]; p.wo = (const u16*)d_in[4];
  p.wg = (const u16*)d_in[6]; p.wu = (const u16*)d_in[7]; p.wd = (const u16*)d_in[8];
  p.out = (u16*)d_out; p.ws = (u64*)d_ws;
  fused_decode<<<256, NTHR, 0, stream>>>(p);
}
```

```cpp
#include <hip/hip_runtime.h>
#include <hip/hip_bf16.h>
#include <stdint.h>
#include <type_traits>

#define AQ 8
#define AW 5
#define AF 2
#define TKV 40.5
#define TQ 3.5
#define TWO 7.0
#define TFF 20.0
#define STAMPS 0

typedef unsigned int u32;
typedef unsigned short u16;
typedef unsigned long long u64;
using u32x4 = __attribute__((ext_vector_type(4))) unsigned;
using u32x2 = __attribute__((ext_vector_type(2))) unsigned;
using f32x4 = __attribute__((ext_vector_type(4))) float;
using bf16x2 = __attribute__((ext_vector_type(2))) __bf16;
using bf16x8 = __attribute__((ext_vector_type(8))) __bf16;
using s16x4 = __attribute__((ext_vector_type(4))) short;
#define GAS __attribute__((address_space(1)))
#define LAS __attribute__((address_space(3)))
#define RLX_AGENT __ATOMIC_RELAXED, __HIP_MEMORY_SCOPE_AGENT
#define RLX_WG __ATOMIC_RELAXED, __HIP_MEMORY_SCOPE_WORKGROUP

constexpr int DM = 576, NH = 32, SK = 8192, DH = 128, FF = 4608, HD = NH * DH;
constexpr int NTHR = 512, W = 32;
constexpr int WS_EPOCH = 0;
constexpr int WS_STAT = 1024;
constexpr int WS_Y = 8192;
constexpr int WS_Q = WS_Y + 2 * 72 * 1024;
constexpr int WS_STAMP = 1 << 20;
constexpr unsigned SPIN_CAP = 1u << 17;
constexpr int L_IMG = 0;
constexpr int L_QP = 147456;
constexpr int L_QRED = L_QP + 4096;
constexpr int L_FRED = L_QRED + 1024;
constexpr int L_AIMG = L_FRED + 1152;
constexpr int L_CTR = L_AIMG + 8 * 272;
constexpr int L_TAG = L_CTR + 8;
constexpr int L_BYTES = L_CTR + 16;

struct Params {
  const u16 *x, *kc, *vc, *wq, *wo, *wg, *wu, *wd;
  u16* out;
  u64* ws;
};

__device__ __forceinline__ float lo16(u32 v) { return __builtin_bit_cast(float, v << 16); }
__device__ __forceinline__ float hi16(u32 v) { return __builtin_bit_cast(float, v & 0xffff0000u); }
__device__ __forceinline__ u32 pkbf(float a, float b) {
  bf16x2 r; r[0] = (__bf16)a; r[1] = (__bf16)b; return __builtin_bit_cast(u32, r);
}
__device__ __forceinline__ float rbf(float f) { return lo16(pkbf(f, f)); }
__device__ __forceinline__ float dot2(u32 k, u32 q, float acc) {
  return __builtin_amdgcn_fdot2_f32_bf16(__builtin_bit_cast(bf16x2, k), __builtin_bit_cast(bf16x2, q), acc, false);
}
template <int CTRL> __device__ __forceinline__ u32 dppu(u32 x) { return (u32)__builtin_amdgcn_mov_dpp((int)x, CTRL, 0xf, 0xf, true); }
template <int CTRL> __device__ __forceinline__ float dpp(float x) { return __builtin_bit_cast(float, dppu<CTRL>(__builtin_bit_cast(u32, x))); }
constexpr int XOR1 = 0xB1, XOR2 = 0x4E, XOR7 = 0x141, XOR8 = 0x128, BCAST = 0x150;
__device__ __forceinline__ float row_sum16(float d) { d += dpp<XOR1>(d); d += dpp<XOR2>(d); d += dpp<XOR7>(d); d += dpp<XOR8>(d); return d; }
__device__ __forceinline__ float row_max16(float d) { d = fmaxf(d, dpp<XOR1>(d)); d = fmaxf(d, dpp<XOR2>(d)); d = fmaxf(d, dpp<XOR7>(d)); d = fmaxf(d, dpp<XOR8>(d)); return d; }
__device__ __forceinline__ float sum16(float a, float b) {
  auto s = __builtin_amdgcn_permlane16_swap(__float_as_uint(a), __float_as_uint(b), false, false);
  return __uint_as_float(s[0]) + __uint_as_float(s[1]);
}
__device__ __forceinline__ float sum32(float a, float b) {
  auto t = __builtin_amdgcn_permlane32_swap(__float_as_uint(a), __float_as_uint(b), false, false);
  return __uint_as_float(t[0]) + __uint_as_float(t[1]);
}
__device__ __forceinline__ float x16_max(float x) {
  auto s = __builtin_amdgcn_permlane16_swap(__float_as_uint(x), __float_as_uint(x), false, false);
  return fmaxf(__uint_as_float(s[0]), __uint_as_float(s[1]));
}
__device__ __forceinline__ float x32_max(float x) {
  auto t = __builtin_amdgcn_permlane32_swap(__float_as_uint(x), __float_as_uint(x), false, false);
  return fmaxf(__uint_as_float(t[0]), __uint_as_float(t[1]));
}
__device__ __forceinline__ float xrow_sum(float x) { x = sum16(x, x); return sum32(x, x); }
__device__ __forceinline__ float wave_sum(float x) { return xrow_sum(row_sum16(x)); }
__device__ __forceinline__ float wave_max(float x) { return x32_max(x16_max(row_max16(x))); }
__device__ __forceinline__ float rdlane(float x, int l) { return __builtin_bit_cast(float, __builtin_amdgcn_readlane(__builtin_bit_cast(int, x), l)); }
__device__ __forceinline__ void rows_reduce_scatter(const float (&a)[8], float& o0, float& o1) {
  const float t0 = sum32(a[0], a[4]), t1 = sum32(a[1], a[5]), t2 = sum32(a[2], a[6]), t3 = sum32(a[3], a[7]);
  o0 = sum16(t0, t2); o1 = sum16(t1, t3);
}

template <int XB> __device__ __forceinline__ void st_gran(GAS u64* g, u64 ep, float v) {
  constexpr u32 M = (1u << XB) - 1u;
  const u32 lo = ((__builtin_bit_cast(u32, v) + (1u << (XB - 1))) & ~M) | ((u32)(ep >> 32) & M);
  __hip_atomic_store(g, (ep << 32) | (u64)lo, RLX_AGENT);
}
__device__ __forceinline__ u64 ld_gran(GAS u64* g) { return __hip_atomic_load(g, RLX_AGENT); }
template <int XB> __device__ __forceinline__ bool gran_ok(u64 g, u64 ep) {
  constexpr u32 M = (1u << XB) - 1u;
  return (u32)(g >> 32) == (u32)ep && ((u32)g & M) == ((u32)(ep >> 32) & M);
}
template <int XB> __device__ __forceinline__ float gran_val(u64 g) { return __builtin_bit_cast(float, (u32)g & ~((1u << XB) - 1u)); }
#define SB() __builtin_amdgcn_sched_barrier(0)
#define STAMP(i)
#define STAMP_FLUSH()

__device__ __forceinline__ void fma8(float (&a)[8], float s, u32x4 v) {
#pragma unroll
  for (int k = 0; k < 4; ++k) { a[2 * k] = fmaf(s, lo16(v[k]), a[2 * k]); a[2 * k + 1] = fmaf(s, hi16(v[k]), a[2 * k + 1]); }
}
__device__ __forceinline__ void pin8(float (&a)[8]) {
  asm volatile("" : "+v"(a[0]), "+v"(a[1]), "+v"(a[2]), "+v"(a[3]), "+v"(a[4]), "+v"(a[5]), "+v"(a[6]), "+v"(a[7]));
}
template <int I, int N> struct SFor {
  template <typename F> static __device__ __forceinline__ void run(F&& f) {
    f(std::integral_constant<int, I>{});
    if constexpr (I + 1 < N) SFor<I + 1, N>::run(f);
  }
};
__device__ __forceinline__ u64 mix64(u64 z) {
  z += 0x9E3779B97F4A7C15ull;
  z = (z ^ (z >> 30)) * 0xBF58476D1CE4E5B9ull;
  z = (z ^ (z >> 27)) * 0x94D049BB133111EBull;
  return z ^ (z >> 31);
}
__device__ __forceinline__ u64 next_epoch(u64 old) {
  const u32 lo = __builtin_amdgcn_readfirstlane((u32)old), hi = __builtin_amdgcn_readfirstlane((u32)(old >> 32));
  u64 e = mix64(((u64)hi << 32) | lo);
  if ((u32)e == lo) e ^= 0x5bd1e995ull;
  return e;
}
__device__ __forceinline__ void pair_dot(u32 va, u32 vb, u32 xp, float& a0, float& a1) {
  a0 = dot2(__builtin_amdgcn_perm(vb, va, 0x05040100u), xp, a0);
  a1 = dot2(__builtin_amdgcn_perm(vb, va, 0x07060302u), xp, a1);
}
__device__ __forceinline__ void aux_sync(LAS unsigned char* L, unsigned target, int lane) {
  asm volatile("s_waitcnt vmcnt(0) lgkmcnt(0)" ::: "memory");
  LAS unsigned* ctr = (LAS unsigned*)(L + L_CTR);
  if (lane == 0) __hip_atomic_fetch_add(ctr, 1u, RLX_WG);
  for (unsigned sp = 0; sp < (1u << 22) && (unsigned)__builtin_amdgcn_readfirstlane(__hip_atomic_load(ctr, RLX_WG)) < target; ++sp) __builtin_amdgcn_s_sleep(1);
  asm volatile("" ::: "memory");
}
__device__ __forceinline__ u32 now16() { return (u32)__builtin_amdgcn_s_memrealtime() * 16u; }
__device__ __forceinline__ void pace(u32& due, u32 tau) {
  while ((int)(now16() - due) < 0) __builtin_amdgcn_s_sleep(1);
  due += tau;
}
__device__ __forceinline__ void resync(u32& due) { const u32 n = now16(); if ((int)(n - due) > 0) due = n; }
constexpr u32 TAU_KV = (u32)(TKV * 1600.0 / 64.0 + 0.5), TAU_Q = (u32)(TQ * 1600.0 / 18.0 + 0.5), TAU_WO = (u32)(TWO * 1600.0 / 36.0 + 0.5), TAU_FF = (u32)(TFF * 1600.0 / 18.0 + 0.5);
__device__ __forceinline__ u32x4 ldbuf(__amdgpu_buffer_rsrc_t rs, int voff, int soff) { return __builtin_amdgcn_raw_buffer_load_b128(rs, voff, soff, 2); }

__global__ void __launch_bounds__(NTHR, 2) fused_decode(Params p) {
  __shared__ __attribute__((aligned(16))) unsigned char lds_raw[L_BYTES];
  LAS unsigned char* L = (LAS unsigned char*)lds_raw;
  const int tid = threadIdx.x, lane = tid & 63, w = __builtin_amdgcn_readfirstlane(tid >> 6), r = lane >> 4, c = lane & 15;
  const int bid = blockIdx.x, xg = bid & 7, j = bid >> 3;
  const int h = xg * 4 + (j >> 3), b = (j >> 2) & 1, s = j & 3;
  const int pi = h * 4 + s;
  const int bh = b * NH + h;
  GAS u64* ws = (GAS u64*)p.ws;
  GAS u64* yg = ws + WS_Y + (size_t)b * (72 * 1024);
  const float LOG2E = 1.4426950408889634f;
  u32 due = now16();
  STAMP(0);
  if (tid == 0) *(LAS unsigned*)(L + L_CTR) = 0u;
  __syncthreads();

  if (w < 4) {
    const int a = w, i8 = j & 7;
    const GAS u32x4* xp = (const GAS u32x4*)(p.x + (lane >> 5) * DM);
    const u32x4 xa = xp[lane & 31], xb = xp[32 + (lane & 31)], xe = xp[64 + (lane & 7)];
    const GAS u32* x32 = (const GAS u32*)p.x;
    const int st3 = (c >> 1) < 2 ? a + 4 * (c >> 1) : 8;
    const u32 xq0 = x32[(c & 1) * (DM / 2) + 36 * i8 + 4 * st3 + r];
    const u32 xc0 = x32[b * (DM / 2) + 72 * a + 4 * c + r];
    const u32 xc1 = x32[b * (DM / 2) + 72 * a + 4 * (16 + (c & 1)) + r];
    u64 ep_old = __hip_atomic_load(ws + WS_EPOCH, RLX_AGENT);
    const GAS u32x4* wqp = (const GAS u32x4*)(p.wq + (size_t)(72 * i8 + 2 * r) * HD + h * DH + c * 8);
    u32x4 wr[6];
    wr[0] = __builtin_nontemporal_load(wqp + (size_t)(8 * a) * (HD / 8)); wr[1] = __builtin_nontemporal_load(wqp + (size_t)(8 * a + 1) * (HD / 8));
    wr[2] = __builtin_nontemporal_load(wqp + (size_t)(8 * a + 32) * (HD / 8)); wr[3] = __builtin_nontemporal_load(wqp + (size_t)(8 * a + 33) * (HD / 8));
    wr[4] = wr[5] = u32x4{0u, 0u, 0u, 0u};
    if (a == 0) { wr[4] = __builtin_nontemporal_load(wqp + (size_t)64 * (HD / 8)); wr[5] = __builtin_nontemporal_load(wqp + (size_t)65 * (HD / 8)); }
    SB();
    u32 P0, P1, PQ;
    {
      float v[24];
#pragma unroll
      for (int k = 0; k < 4; ++k) {
        v[2 * k] = lo16(xa[k]); v[2 * k + 1] = hi16(xa[k]); v[8 + 2 * k] = lo16(xb[k]); v[8 + 2 * k + 1] = hi16(xb[k]);
        v[16 + 2 * k] = lo16(xe[k]); v[16 + 2 * k + 1] = hi16(xe[k]);
      }
      float s0 = 0.f, s1 = 0.f, s2 = 0.f;
#pragma unroll
      for (int k = 0; k < 8; ++k) { s0 += v[k]; s1 += v[8 + k]; s2 += v[16 + k]; }
      const bool t8 = (lane & 31) < 8;
      float mean = row_sum16(s0 + s1 + (t8 ? s2 : 0.f)); mean = sum16(mean, mean) * (1.0f / DM);
      float q0 = 0.f, q1 = 0.f, q2 = 0.f;
#pragma unroll
      for (int k = 0; k < 8; ++k) { float d0 = v[k] - mean, d1 = v[8 + k] - mean, d2 = v[16 + k] - mean; q0 = fmaf(d0, d0, q0); q1 = fmaf(d1, d1, q1); q2 = fmaf(d2, d2, q2); }
      float var = row_sum16(q0 + q1 + (t8 ? q2 : 0.f)); var = sum16(var, var) * (1.0f / DM);
      const float mbv = rbf(mean), denv = rbf(sqrtf(rbf(rbf(var) + 1e-5f)));
      const float mb0 = rdlane(mbv, 0), mb1 = rdlane(mbv, 32), dn0 = rdlane(denv, 0), dn1 = rdlane(denv, 32);
      const float mq = (c & 1) ? mb1 : mb0, dq = (c & 1) ? dn1 : dn0;
      PQ = pkbf(rbf(lo16(xq0) - mq) / dq, rbf(hi16(xq0) - mq) / dq);
      const float mb = b ? mb1 : mb0, den = b ? dn1 : dn0;
      P0 = pkbf(rbf(lo16(xc0) - mb) / den, rbf(hi16(xc0) - mb) / den);
      P1 = pkbf(rbf(lo16(xc1) - mb) / den, rbf(hi16(xc1) - mb) / den);
    }
    const u64 ep = next_epoch(ep_old);
    STAMP(1);
    {
      float qa[8] = {0, 0, 0, 0, 0, 0, 0, 0}, qb[8] = {0, 0, 0, 0, 0, 0, 0, 0};
      SFor<0, 3>::run([&](auto mc) {
        constexpr int m = decltype(mc)::value;
        const u32 x0 = dppu<BCAST + 2 * m>(PQ), x1 = dppu<BCAST + 2 * m + 1>(PQ);
#pragma unroll
        for (int k = 0; k < 4; ++k) {
          const u32 pl = __builtin_amdgcn_perm(wr[2 * m + 1][k], wr[2 * m][k], 0x05040100u), ph = __builtin_amdgcn_perm(wr[2 * m + 1][k], wr[2 * m][k], 0x07060302u);
          qa[2 * k] = dot2(pl, x0, qa[2 * k]); qa[2 * k + 1] = dot2(ph, x0, qa[2 * k + 1]);
          qb[2 * k] = dot2(pl, x1, qb[2 * k]); qb[2 * k + 1] = dot2(ph, x1, qb[2 * k + 1]);
        }
      });
      float o0, o1, o2, o3;
      rows_reduce_scatter(qa, o0, o1);
      rows_reduce_scatter(qb, o2, o3);
      LAS float* qp = (LAS float*)(L + L_QP) + a * 256 + c * 8 + 4 * (r >> 1) + 2 * (r & 1);
      *(LAS u32x2*)qp = u32x2{__float_as_uint(o0), __float_as_uint(o1)};
      *(LAS u32x2*)(qp + 128) = u32x2{__float_as_uint(o2), __float_as_uint(o3)};
      if (tid == 0) *(LAS u64*)(L + L_TAG) = ep;
      aux_sync(L, 4, lane);
      LAS float* qs = (LAS float*)(L + L_QP) + tid;
      const float part = (qs[0] + qs[256]) + (qs[512] + qs[768]);
      st_gran<4>(ws + WS_Q + ((h * 2 + (tid >> 7)) * 8 + i8) * 128 + (tid & 127), ep, part);
      STAMP(7);
      GAS u64* g = ws + WS_Q + ((h * 2 + b) * 8 + 4 * (tid >> 7)) * 128 + (tid & 127);
      u64 v[4];
      bool bad = false;
      for (unsigned spins = 0;; ++spins) {
        bool ok = true;
#pragma unroll
        for (int k = 0; k < 4; ++k) v[k] = ld_gran(g + k * 128);
#pragma unroll
        for (int k = 0; k < 4; ++k) ok &= gran_ok<4>(v[k], ep);
        if (__all(ok)) break;
        if (spins >= SPIN_CAP) { bad = true; break; }
      }
      float qsum = (gran_val<4>(v[0]) + gran_val<4>(v[1])) + (gran_val<4>(v[2]) + gran_val<4>(v[3]));
      if (bad) qsum = __builtin_nanf("");
      ((LAS float*)(L + L_QRED))[tid] = qsum;
    }
    __syncthreads();
    STAMP(2);
    {
      const GAS u32* src = (const GAS u32*)(p.wo + (size_t)(h * DH + 32 * a) * DM) + lane * 4;
      resync(due);
      SFor<0, 36>::run([&](auto ic) {
        constexpr int i = decltype(ic)::value;
        pace(due, TAU_WO);
        __builtin_amdgcn_global_load_lds(src + i * 256, (LAS u32*)(L + L_IMG + a * 36864 + i * 1024), 16, 0, 0);
        if constexpr (i >= AW) asm volatile("s_waitcnt vmcnt(%0)" ::"n"(AW) : "memory");
      });
    }
    aux_sync(L, 8, lane);
    STAMP(3);
    u32x2 bf[9][4][2];
    {
      LAS unsigned char* tb = L + L_IMG + (8 * r + (c >> 2)) * 1152 + (144 * a + 4 * (c & 3)) * 2;
#pragma unroll
      for (int t = 0; t < 9; ++t)
#pragma unroll
        for (int ks = 0; ks < 4; ++ks)
#pragma unroll
          for (int u = 0; u < 2; ++u)
            bf[t][ks][u] = __builtin_bit_cast(u32x2, __builtin_amdgcn_ds_read_tr16_b64_v4i16((LAS s16x4*)(tb + (32 * ks + 4 * u) * 1152 + t * 32)));
#pragma unroll
      for (int t = 0; t < 9; ++t)
#pragma unroll
        for (int ks = 0; ks < 4; ++ks) asm volatile("" : "+v"(bf[t][ks][0]), "+v"(bf[t][ks][1]));
    }
    SB();
    STAMP(4);
    u32x4 dw[11];
    {
      const int cc = c < 9 ? c : 8;
      const size_t go = (size_t)(144 * a + 2 * r) * FF + pi * 36 + cc * 4;
      const GAS u32x2* gp = (const GAS u32x2*)(p.wg + go);
      const GAS u32x2* up = (const GAS u32x2*)(p.wu + go);
      u32x2 fr[AF][4];
      resync(due);
#pragma unroll
      for (int m = 0; m < AF; ++m) {
        pace(due, TAU_FF);
        fr[m][0] = gp[(size_t)(8 * m) * (FF / 4)]; fr[m][1] = gp[(size_t)(8 * m + 1) * (FF / 4)];
        fr[m][2] = up[(size_t)(8 * m) * (FF / 4)]; fr[m][3] = up[(size_t)(8 * m + 1) * (FF / 4)];
      }
      SB();
      float fa[8] = {0, 0, 0, 0, 0, 0, 0, 0};
      SFor<0, 18>::run([&](auto mc) {
        constexpr int m = decltype(mc)::value, i = m % AF;
        const u32 xq = m < 16 ? dppu<BCAST + (m & 15)>(P0) : dppu<BCAST + (m & 15)>(P1);
        pair_dot(fr[i][0][0], fr[i][1][0], xq, fa[0], fa[1]); pair_dot(fr[i][0][1], fr[i][1][1], xq, fa[2], fa[3]);
        pair_dot(fr[i][2][0], fr[i][3][0], xq, fa[4], fa[5]); pair_dot(fr[i][2][1], fr[i][3][1], xq, fa[6], fa[7]);
        pin8(fa);
        SB();
        if constexpr (m + AF < 18) {
          pace(due, TAU_FF);
          fr[i][0] = gp[(size_t)(8 * (m + AF)) * (FF / 4)]; fr[i][1] = gp[(size_t)(8 * (m + AF) + 1) * (FF / 4)];
          fr[i][2] = up[(size_t)(8 * (m + AF)) * (FF / 4)]; fr[i][3] = up[(size_t)(8 * (m + AF) + 1) * (FF / 4)];
        }
        SB();
      });
      const GAS u32x4* dp = (const GAS u32x4*)(p.wd + (size_t)(pi * 36 + 9 * a) * DM);
#pragma unroll
      for (int i = 0; i < 9; ++i) dw[i] = dp[i * 72 + lane];
      dw[9] = dp[(lane >> 3) * 72 + 64 + (lane & 7)];
      dw[10] = dp[8 * 72 + 64 + (lane & 7)];
      SB();
      float o0, o1;
      rows_reduce_scatter(fa, o0, o1);
      LAS float* fred = (LAS float*)(L + L_FRED);
      if (c < 9) *(LAS u32x2*)(fred + a * 72 + 36 * (r >> 1) + 4 * c + 2 * (r & 1)) = u32x2{__float_as_uint(o0), __float_as_uint(o1)};
    }
    aux_sync(L, 12, lane);
    STAMP(5);
    {
      LAS float* fred = (LAS float*)(L + L_FRED);
      float hh[2];
#pragma unroll
      for (int e = 0; e < 2; ++e) {
        const int hr = 9 * a + (e ? 8 : (lane >> 3));
        float gs = fred[(lane & 3) * 72 + hr], us = fred[(lane & 3) * 72 + 36 + hr];
        gs += dpp<XOR1>(gs); gs += dpp<XOR2>(gs);
        us += dpp<XOR1>(us); us += dpp<XOR2>(us);
        gs = rbf(gs); us = rbf(us);
        const float inner = 0.7978845608028654f * (gs + 0.044715f * gs * gs * gs);
        const float ge = rbf(gs / (1.0f + __expf(-2.0f * inner)));
        hh[e] = rbf(ge * us);
      }
      float ym[8] = {0, 0, 0, 0, 0, 0, 0, 0}, yt[8] = {0, 0, 0, 0, 0, 0, 0, 0};
#pragma unroll
      for (int i = 0; i < 8; ++i) fma8(ym, rdlane(hh[0], 8 * i), dw[i]);
      fma8(ym, hh[1], dw[8]);
      fma8(yt, hh[0], dw[9]);
      fma8(yt, lane < 8 ? hh[1] : 0.f, dw[10]);
#pragma unroll
      for (int k = 0; k < 8; ++k) { float t = yt[k]; t += dpp<XOR8>(t); yt[k] = xrow_sum(t); }
      LAS float* yf = (LAS float*)(L + L_IMG) + a * 576;
      *(LAS f32x4*)(yf + lane * 8) = f32x4{ym[0], ym[1], ym[2], ym[3]};
      *(LAS f32x4*)(yf + lane * 8 + 4) = f32x4{ym[4], ym[5], ym[6], ym[7]};
      if (lane < 8) { *(LAS f32x4*)(yf + 512 + lane * 8) = f32x4{yt[0], yt[1], yt[2], yt[3]}; *(LAS f32x4*)(yf + 512 + lane * 8 + 4) = f32x4{yt[4], yt[5], yt[6], yt[7]}; }
    }
    aux_sync(L, 16, lane);
    float yfs[3];
    {
      LAS float* yf = (LAS float*)(L + L_IMG) + 144 * a;
#pragma unroll
      for (int t = 0; t < 3; ++t) { const int o = t < 2 ? 64 * t + lane : 128 + c; yfs[t] = (yf[o] + yf[576 + o]) + (yf[1152 + o] + yf[1728 + o]); }
    }
    asm volatile("" : "+v"(yfs[0]), "+v"(yfs[1]), "+v"(yfs[2]));
    STAMP(6);
    __syncthreads();
    STAMP(8);
    {
      u32x4 af[4];
#pragma unroll
      for (int ks = 0; ks < 4; ++ks) af[ks] = *(LAS u32x4*)(L + L_AIMG + (lane & 7) * 272 + ks * 64 + r * 16);
      f32x4 d[9];
#pragma unroll
      for (int t = 0; t < 9; ++t) d[t] = f32x4{0.f, 0.f, 0.f, 0.f};
#pragma unroll
      for (int ks = 0; ks < 4; ++ks)
#pragma unroll
        for (int t = 0; t < 9; ++t) {
          const u32x4 bb = {bf[t][ks][0][0], bf[t][ks][0][1], bf[t][ks][1][0], bf[t][ks][1][1]};
          d[t] = __builtin_amdgcn_mfma_f32_16x16x32_bf16(__builtin_bit_cast(bf16x8, af[ks]), __builtin_bit_cast(bf16x8, bb), d[t], 0, 0, 0);
        }
      float e[9];
#pragma unroll
      for (int t = 0; t < 9; ++t) e[t] = (d[t][0] + d[t][1]) + (d[t][2] + d[t][3]);
      const bool lo32 = lane < 32;
      const float s01 = sum16(e[0], e[1]), s23 = sum16(e[2], e[3]), s45 = sum16(e[4], e[5]), s67 = sum16(e[6], e[7]), s8 = sum16(e[8], e[8]);
      const float y0 = (lo32 ? s01 : s23) + yfs[0], y1 = (lo32 ? s45 : s67) + yfs[1], y2 = s8 + yfs[2];
      STAMP(9);
      GAS u64* yo = yg + (18 * a + (lane >> 3)) * 1024 + pi * 8 + (lane & 7);
      st_gran<8>(yo, ep, y0);
      st_gran<8>(yo + 8 * 1024, ep, y1);
      if (lane < 16) st_gran<8>(yo + 16 * 1024, ep, y2);
    }
    STAMP(10);
  } else {
    const int sw = w - 4;
    const u16 xres = ((const GAS u16*)p.x)[b * DM + (pi < 72 ? pi : 0) * 8 + (lane & 7)];
    const size_t kvoff = ((size_t)bh * SK + s * 2048) * DH;
    const u16* kb = p.kc + kvoff;
    const u16* vb = p.vc + kvoff;
    const int voff = sw * 1024 + lane * 16;
    u32x4 ring[W];
    {
      const __amdgpu_buffer_rsrc_t rk = __builtin_amdgcn_make_buffer_rsrc((void*)kb, 0, 2048 * 256, 0x00020000);
#pragma unroll
      for (int i = 0; i < W; ++i) { if (i % 4 == 0) pace(due, TAU_KV); ring[i] = ldbuf(rk, voff, i * 4096); SB(); }
    }
    SB();
    STAMP(1);
    __syncthreads();
    u32 q[4];
    {
      LAS float* qred = (LAS float*)(L + L_QRED);
      f32x4 t0 = *(LAS f32x4*)(qred + c * 8), t1 = *(LAS f32x4*)(qred + c * 8 + 4);
      t0 += *(LAS f32x4*)(qred + 128 + c * 8); t1 += *(LAS f32x4*)(qred + 128 + c * 8 + 4);
      q[0] = pkbf(t0[0], t0[1]); q[1] = pkbf(t0[2], t0[3]); q[2] = pkbf(t1[0], t1[1]); q[3] = pkbf(t1[2], t1[3]);
    }
    resync(due);
    STAMP(2);
    float sc[8] = {0, 0, 0, 0, 0, 0, 0, 0};
    const float SCALE = 0.08838834764831845f;
#pragma nounroll
    for (int it = 0; it < 4; ++it) {
      const __amdgpu_buffer_rsrc_t rn = __builtin_amdgcn_make_buffer_rsrc((void*)(it == 3 ? vb : kb + (it + 1) * (W * 2048)), 0, W * 4096, 0x00020000);
      float keep0 = 0.f, keep1 = 0.f;
      SFor<0, W>::run([&](auto tc) {
        constexpr int J = decltype(tc)::value;
        float d = dot2(ring[J][0], q[0], 0.f);
        d = dot2(ring[J][1], q[1], d); d = dot2(ring[J][2], q[2], d); d = dot2(ring[J][3], q[3], d);
        asm volatile("" : "+v"(d));
        if constexpr (J % 4 == 0) pace(due, TAU_KV);
        SB();
        ring[J] = ldbuf(rn, voff, J * 4096);
        d = row_sum16(d);
        if constexpr (J < 16) keep0 = (c == J) ? d : keep0; else keep1 = (c == J - 16) ? d : keep1;
        SB();
      });
#pragma unroll
      for (int k = 0; k < 6; ++k) sc[k] = sc[k + 2];
      sc[6] = rbf(rbf(keep0) * SCALE); sc[7] = rbf(rbf(keep1) * SCALE);
    }
    STAMP(4);
    const u32x2 epv = *(LAS u32x2*)(L + L_TAG);
    const u64 ep = ((u64)__builtin_amdgcn_readfirstlane(epv[1]) << 32) | (u32)__builtin_amdgcn_readfirstlane(epv[0]);
    const float mw = wave_max(fmaxf(fmaxf(fmaxf(sc[0], sc[1]), fmaxf(sc[2], sc[3])), fmaxf(fmaxf(sc[4], sc[5]), fmaxf(sc[6], sc[7]))));
#pragma unroll
    for (int k = 0; k < 8; ++k) sc[k] = __builtin_amdgcn_exp2f((sc[k] - mw) * LOG2E);
    {
      const float lw = wave_sum(((sc[0] + sc[1]) + (sc[2] + sc[3])) + ((sc[4] + sc[5]) + (sc[6] + sc[7])));
      if (lane < 2) st_gran<8>(ws + WS_STAT + bh * 32 + lane * 16 + s * 4 + sw, ep, lane ? lw : mw);
    }
    SB();
    float acc[8] = {0, 0, 0, 0, 0, 0, 0, 0};
#pragma nounroll
    for (int it = 0; it < 3; ++it) {
      const __amdgpu_buffer_rsrc_t rn = __builtin_amdgcn_make_buffer_rsrc((void*)(vb + (it + 1) * (W * 2048)), 0, W * 4096, 0x00020000);
      SFor<0, W>::run([&](auto tc) {
        constexpr int J = decltype(tc)::value;
        fma8(acc, dpp<BCAST + (J & 15)>(sc[J / 16]), ring[J]);
        pin8(acc);
        if constexpr (J % 4 == 0) pace(due, TAU_KV);
        SB();
        ring[J] = ldbuf(rn, voff, J * 4096);
        SB();
      });
#pragma unroll
      for (int k = 0; k < 6; ++k) sc[k] = sc[k + 2];
    }
    STAMP(6);
    GAS u64* sgp = ws + WS_STAT + bh * 32 + (lane & 31);
    u64 sg = ld_gran(sgp);
    SB();
    SFor<0, W>::run([&](auto tc) {
      constexpr int J = decltype(tc)::value;
      fma8(acc, dpp<BCAST + (J & 15)>(sc[J / 16]), ring[J]);
      pin8(acc);
      SB();
    });
    STAMP(7);
    bool bad = false;
    for (unsigned spins = 0; !__all(gran_ok<8>(sg, ep)); ++spins) {
      if (spins >= SPIN_CAP) { bad = true; break; }
      __builtin_amdgcn_s_sleep(2);
      sg = ld_gran(sgp);
    }
    {
      const u32 sv = __float_as_uint(gran_val<8>(sg));
      auto t = __builtin_amdgcn_permlane16_swap(sv, sv, false, false);
      const float mi = __uint_as_float(t[0]), li = __uint_as_float(t[1]);
      const float M = row_max16(mi);
      const float Ls = row_sum16(li * __builtin_amdgcn_exp2f((mi - M) * LOG2E));
      float f = __builtin_amdgcn_exp2f((mw - M) * LOG2E) * __builtin_amdgcn_rcpf(Ls);
      if (bad) f = __builtin_nanf("");
      float o0, o1;
      rows_reduce_scatter(acc, o0, o1);
      o0 *= f; o1 *= f;
      const u32 hi = pkbf(o0, o1);
      const u32 lo = pkbf(o0 - lo16(hi), o1 - hi16(hi));
      LAS u32* ar = (LAS u32*)(L + L_AIMG + (2 * sw) * 272 + (c * 8 + 4 * (r >> 1) + 2 * (r & 1)) * 2);
      ar[0] = hi; ar[68] = lo;
    }
    __syncthreads();
    STAMP(8);
    if (sw == 0 && pi < 72) {
      GAS u64* g = yg + pi * 1024 + lane;
      u64 v[16];
#pragma unroll
      for (int k = 0; k < 16; ++k) v[k] = ld_gran(g + k * 64);
      bool bad2 = false;
      for (unsigned spins = 0;; ++spins) {
        bool ok = true;
#pragma unroll
        for (int k = 0; k < 16; ++k) ok &= gran_ok<8>(v[k], ep);
        if (__all(ok)) break;
        if (spins >= SPIN_CAP) { bad2 = true; break; }
        __builtin_amdgcn_s_sleep(2);
#pragma unroll
        for (int k = 0; k < 16; ++k) if (!gran_ok<8>(v[k], ep)) v[k] = ld_gran(g + k * 64);
      }
      float t = 0.f;
#pragma unroll
      for (int k = 0; k < 16; ++k) t += gran_val<8>(v[k]);
      t += dpp<XOR8>(t);
      t = xrow_sum(t);
      float res = __builtin_bit_cast(float, (u32)xres << 16) + t;
      if (bad2) res = __builtin_nanf("");
      if (lane < 8) ((GAS u16*)p.out)[b * DM + pi * 8 + lane] = (u16)(pkbf(res, res) & 0xffffu);
      if (pi == 0 && b == 0 && lane == 0) __hip_atomic_store(ws + WS_EPOCH, ep, RLX_AGENT);
    }
  }
  STAMP(11);
  STAMP_FLUSH();
}

extern "C" void kernel_launch(void* const* d_in, const int* in_sizes, int n_in, void* d_out, int out_size, void* d_ws, size_t ws_size, hipStream_t stream) {
  Params p;
  p.x = (const u16*)d_in[0]; p.kc = (const u16*)d_in[1]; p.vc = (const u16*)d_in[2]; p.wq = (const u16*)d_in[3]; p.wo = (const u16*)d_in[4];
  p.wg = (const u16*)d_in[6]; p.wu = (const u16*)d_in[7]; p.wd = (const u16*)d_in[8];
  p.out = (u16*)d_out; p.ws = (u64*)d_ws;
  fused_decode<<<256, NTHR, 0, stream>>>(p);
}
```

```cpp
#include <hip/hip_runtime.h>
#include <hip/hip_bf16.h>
#include <stdint.h>
#include <type_traits>

#define TG0 0.58
#define TG 0.556
#define GAP_G 0.64
#define GAP_O 0.82
#define GAP_D 0.64
#define TQG 5.5
#define THG 0.9
#define TWO 0.28
#define RDELAY 0.75
#define LB 3
#define VOFF 20
#define NP 2
#define NL 8
#define CODEKB 32

typedef unsigned int u32;
typedef unsigned short u16;
typedef unsigned long long u64;
using u32x4 = __attribute__((ext_vector_type(4))) unsigned;
using u32x2 = __attribute__((ext_vector_type(2))) unsigned;
using f32x4 = __attribute__((ext_vector_type(4))) float;
using bf16x2 = __attribute__((ext_vector_type(2))) __bf16;
using bf16x8 = __attribute__((ext_vector_type(8))) __bf16;
using s16x4 = __attribute__((ext_vector_type(4))) short;
#define GAS __attribute__((address_space(1)))
#define LAS __attribute__((address_space(3)))
#define RLX_AGENT __ATOMIC_RELAXED, __HIP_MEMORY_SCOPE_AGENT
#define RLX_WG __ATOMIC_RELAXED, __HIP_MEMORY_SCOPE_WORKGROUP

constexpr int DM = 576, NH = 32, SK = 8192, DH = 128, FF = 4608, HD = NH * DH;
constexpr int NTHR = 512, W = 32;
constexpr int WS_EPOCH = 0;
constexpr int WS_Y = 8192;
constexpr int WS_Q = WS_Y + 2 * 72 * 1024;
constexpr int WS_H = WS_Q + 32 * 8 * 128;
constexpr int WS_END = WS_H + 12 * 4608;
constexpr int WS_STAT = WS_END;
constexpr int WS_QF = WS_STAT + 64 * 64;
constexpr int WS_HF = WS_QF + 256 * 16;
constexpr int WS_HG = WS_HF + 432 * 16;
constexpr unsigned SPIN_CAP = 1u << 17;
constexpr int L_IMG = 0;
constexpr int L_YF = 147456;
constexpr int L_QP = L_YF;
constexpr int L_QRED = L_YF + 9216;
constexpr int L_FRED = L_QRED + 1024;
constexpr int L_AIMG = L_FRED + 2048;
constexpr int L_CTR = L_AIMG + 8 * 272;
constexpr int L_TAG = L_CTR + 16;
constexpr int L_ML = L_CTR + 32;
constexpr int L_BYTES = L_CTR + 64;
static_assert(L_BYTES <= 163840, "LDS");

struct Params {
  const u16 *x, *kc, *vc, *wq, *wo, *wg, *wu, *wd;
  u16* out;
  u64* ws;
};

__device__ __forceinline__ float lo16(u32 v) { return __builtin_bit_cast(float, v << 16); }
__device__ __forceinline__ float hi16(u32 v) { return __builtin_bit_cast(float, v & 0xffff0000u); }
__device__ __forceinline__ u32 pkbf(float a, float b) {
  bf16x2 r; r[0] = (__bf16)a; r[1] = (__bf16)b; return __builtin_bit_cast(u32, r);
}
__device__ __forceinline__ float rbf(float f) { return lo16(pkbf(f, f)); }
__device__ __forceinline__ float dot2(u32 k, u32 q, float acc) {
  return __builtin_amdgcn_fdot2_f32_bf16(__builtin_bit_cast(bf16x2, k), __builtin_bit_cast(bf16x2, q), acc, false);
}
template <int CTRL> __device__ __forceinline__ u32 dppu(u32 x) { return (u32)__builtin_amdgcn_mov_dpp((int)x, CTRL, 0xf, 0xf, true); }
template <int CTRL> __device__ __forceinline__ float dpp(float x) { return __builtin_bit_cast(float, dppu<CTRL>(__builtin_bit_cast(u32, x))); }
constexpr int XOR1 = 0xB1, XOR2 = 0x4E, XOR7 = 0x141, XOR8 = 0x128, BCAST = 0x150;
__device__ __forceinline__ float row_sum16(float d) { d += dpp<XOR1>(d); d += dpp<XOR2>(d); d += dpp<XOR7>(d); d += dpp<XOR8>(d); return d; }
__device__ __forceinline__ float row_max16(float d) { d = fmaxf(d, dpp<XOR1>(d)); d = fmaxf(d, dpp<XOR2>(d)); d = fmaxf(d, dpp<XOR7>(d)); d = fmaxf(d, dpp<XOR8>(d)); return d; }
__device__ __forceinline__ float sum16(float a, float b) {
  auto s = __builtin_amdgcn_permlane16_swap(__float_as_uint(a), __float_as_uint(b), false, false);
  return __uint_as_float(s[0]) + __uint_as_float(s[1]);
}
__device__ __forceinline__ float sum32(float a, float b) {
  auto t = __builtin_amdgcn_permlane32_swap(__float_as_uint(a), __float_as_uint(b), false, false);
  return __uint_as_float(t[0]) + __uint_as_float(t[1]);
}
__device__ __forceinline__ float x16_max(float x) {
  auto s = __builtin_amdgcn_permlane16_swap(__float_as_uint(x), __float_as_uint(x), false, false);
  return fmaxf(__uint_as_float(s[0]), __uint_as_float(s[1]));
}
__device__ __forceinline__ float x32_max(float x) {
  auto t = __builtin_amdgcn_permlane32_swap(__float_as_uint(x), __float_as_uint(x), false, false);
  return fmaxf(__uint_as_float(t[0]), __uint_as_float(t[1]));
}
__device__ __forceinline__ float xrow_sum(float x) { x = sum16(x, x); return sum32(x, x); }
__device__ __forceinline__ float wave_sum(float x) { return xrow_sum(row_sum16(x)); }
__device__ __forceinline__ float wave_max(float x) { return x32_max(x16_max(row_max16(x))); }
__device__ __forceinline__ float rdlane(float x, int l) { return __builtin_bit_cast(float, __builtin_amdgcn_readlane(__builtin_bit_cast(int, x), l)); }
__device__ __forceinline__ void rows_reduce_scatter(const float (&a)[8], float& o0, float& o1) {
  const float t0 = sum32(a[0], a[4]), t1 = sum32(a[1], a[5]), t2 = sum32(a[2], a[6]), t3 = sum32(a[3], a[7]);
  o0 = sum16(t0, t2); o1 = sum16(t1, t3);
}

template <int XB> __device__ __forceinline__ void st_gran(GAS u64* g, u64 ep, float v) {
  constexpr u32 M = (1u << XB) - 1u;
  const u32 lo = ((__builtin_bit_cast(u32, v) + (1u << (XB - 1))) & ~M) | ((u32)(ep >> 32) & M);
  __hip_atomic_store(g, (ep << 32) | (u64)lo, RLX_AGENT);
}
__device__ __forceinline__ u64 ld_gran(GAS u64* g) { return __hip_atomic_load(g, RLX_AGENT); }
template <int XB> __device__ __forceinline__ bool gran_ok(u64 g, u64 ep) {
  constexpr u32 M = (1u << XB) - 1u;
  return (u32)(g >> 32) == (u32)ep && ((u32)g & M) == ((u32)(ep >> 32) & M);
}
template <int XB> __device__ __forceinline__ float gran_val(u64 g) { return __builtin_bit_cast(float, (u32)g & ~((1u << XB) - 1u)); }
__device__ __forceinline__ u32 gran_lo(u64 ep, float v) { return ((__builtin_bit_cast(u32, v) + 0x80u) & 0xffffff00u) | ((u32)(ep >> 32) & 0xffu); }
__device__ __forceinline__ int pair_col(int P, int e) { const int a = P / 72, l = P % 72; return 144 * a + (l < 64 ? l + 64 * e : 64 + l + 8 * e); }
#define SB() __builtin_amdgcn_sched_barrier(0)

__device__ __forceinline__ void fma8(float (&a)[8], float s, u32x4 v) {
#pragma unroll
  for (int k = 0; k < 4; ++k) { a[2 * k] = fmaf(s, lo16(v[k]), a[2 * k]); a[2 * k + 1] = fmaf(s, hi16(v[k]), a[2 * k + 1]); }
}
__device__ __forceinline__ void pin8(float (&a)[8]) {
  asm volatile("" : "+v"(a[0]), "+v"(a[1]), "+v"(a[2]), "+v"(a[3]), "+v"(a[4]), "+v"(a[5]), "+v"(a[6]), "+v"(a[7]));
}
template <int I, int N> struct SFor {
  template <typename F> static __device__ __forceinline__ void run(F&& f) {
    f(std::integral_constant<int, I>{});
    if constexpr (I + 1 < N) SFor<I + 1, N>::run(f);
  }
};
__device__ __forceinline__ u64 mix64(u64 z) {
  z += 0x9E3779B97F4A7C15ull;
  z = (z ^ (z >> 30)) * 0xBF58476D1CE4E5B9ull;
  z = (z ^ (z >> 27)) * 0x94D049BB133111EBull;
  return z ^ (z >> 31);
}
__device__ __forceinline__ u64 next_epoch(u64 old) {
  u64 e = mix64(old);
  if ((u32)e == (u32)old) e ^= 0x5bd1e995ull;
  asm volatile("" : "+s"(e));
  return e;
}
__device__ __forceinline__ void lds_arrive(LAS unsigned char* L, int which, int lane) {
  asm volatile("s_waitcnt lgkmcnt(0)" ::: "memory");
  if (lane == 0) __hip_atomic_fetch_add((LAS unsigned*)(L + L_CTR) + which, 1u, RLX_WG);
}
__device__ __forceinline__ void lds_wait(LAS unsigned char* L, int which, unsigned target) {
  LAS unsigned* ctr = (LAS unsigned*)(L + L_CTR) + which;
#pragma nounroll
  for (unsigned sp = 0; sp < (1u << 22) && (unsigned)__builtin_amdgcn_readfirstlane(__hip_atomic_load(ctr, RLX_WG)) < target; ++sp) __builtin_amdgcn_s_sleep(1);
  asm volatile("" ::: "memory");
}
__device__ __forceinline__ void aux_sync(LAS unsigned char* L, unsigned target, int lane) { lds_arrive(L, 0, lane); lds_wait(L, 0, target); }
struct Pacer { u32 due; };
__device__ __forceinline__ u32 now16() { return (u32)__builtin_amdgcn_s_memrealtime() * 16u; }
__device__ __forceinline__ int wait_until(u32 t) {
  int late;
  asm volatile(
      "1:\n\t"
      "s_memrealtime vcc\n\t"
      "s_waitcnt lgkmcnt(0)\n\t"
      "s_lshl_b32 vcc_lo, vcc_lo, 4\n\t"
      "s_sub_i32 %0, vcc_lo, %1\n\t"
      "s_cmp_ge_i32 %0, 0\n\t"
      "s_cbranch_scc1 2f\n\t"
      "s_sleep 1\n\t"
      "s_branch 1b\n"
      "2:"
      : "=&s"(late) : "s"(t) : "scc", "vcc", "memory");
  return late;
}
__device__ __forceinline__ void pace(Pacer& P, u32 tau) { wait_until(P.due); P.due += tau; }
constexpr u32 US(double x) { return (u32)(x * 1600.0 + 0.5); }
constexpr u32 TAU0 = US(TG0), TAU = US(TG);
constexpr u32 T_B2 = 16 * TAU0, T_B3 = T_B2 + US(GAP_G) + 8 * TAU, T_B4 = T_B3 + US(GAP_G) + 8 * TAU, T_B5 = T_B4 + US(GAP_O) + 8 * TAU;
__device__ __forceinline__ void dma16_nt(const void* base, u32 voff, u32 dst) {
  unsigned keep;
  asm volatile("s_mov_b32 %0, m0\n\ts_mov_b32 m0, %3\n\ts_nop 0\n\tglobal_load_lds_dwordx4 %1, %2 nt\n\ts_mov_b32 m0, %0" : "=&s"(keep) : "v"(voff), "s"(base), "s"(dst) : "memory");
}
__device__ __forceinline__ void dma16(const void* base, u32 voff, u32 dst) {
  unsigned keep;
  asm volatile("s_mov_b32 %0, m0\n\ts_mov_b32 m0, %3\n\ts_nop 0\n\tglobal_load_lds_dwordx4 %1, %2\n\ts_mov_b32 m0, %0" : "=&s"(keep) : "v"(voff), "s"(base), "s"(dst) : "memory");
}
constexpr int ITS = 8 * 32768;
__host__ __device__ constexpr int soff(int J) { return ((J >> LB) << (LB + 14)) + ((J & ((1 << LB) - 1)) << 12); }
constexpr int ITB = soff(31) + 4096;
template <int AUX> __device__ __forceinline__ u32x4 ldbuf(__amdgpu_buffer_rsrc_t rs, int voff, int soff) { return __builtin_amdgcn_raw_buffer_load_b128(rs, voff, soff, AUX); }

__global__ void __launch_bounds__(NTHR, 2) fused_decode(Params p) {
  __shared__ __attribute__((aligned(16))) unsigned char lds_raw[L_BYTES];
  LAS unsigned char* L = (LAS unsigned char*)lds_raw;
  const int tid = threadIdx.x, lane = tid & 63, w = __builtin_amdgcn_readfirstlane(tid >> 6), r = lane >> 4, c = lane & 15;
  const int bid = blockIdx.x, xg = bid & 7, j = bid >> 3;
  const int h = xg + 8 * (j >> 3), b = (j >> 2) & 1, s = j & 3;
  const int pi = xg * 16 + (j >> 3) * 4 + s;
  const int bh = b * NH + h;
  GAS u64* ws = (GAS u64*)p.ws;
  const float LOG2E = 1.4426950408889634f;
  Pacer P;
  {
    u64 pc;
    asm volatile("s_getpc_b64 %0" : "=s"(pc));
    const int chunk = j + 32 * w;
    if (chunk < CODEKB) dma16((const void*)((pc & ~255ull) + chunk * 1024), lane * 16, (u32)(size_t)(L + L_YF) + 4096 + w * 1024);
  }
  const int sw = w & 3;
  const size_t kvoff = ((size_t)bh * SK + s * (16 << LB)) * DH;
  const u16* kb = p.kc + kvoff;
  const int voff = sw * 1024 + lane * 16;
  u32x4 wr[6];
  if (w < 4) {
    const u16* wqb = p.wq + (size_t)(72 * (j & 7)) * HD + h * DH;
    const __amdgpu_buffer_rsrc_t rq = __builtin_amdgcn_make_buffer_rsrc((void*)(wqb + 8 * sw * HD), 0, 40 * HD * 2, 0x00020000);
    const __amdgpu_buffer_rsrc_t rq2 = __builtin_amdgcn_make_buffer_rsrc((void*)(wqb + 64 * HD), 0, w == 0 ? 8 * HD * 2 : 0, 0x00020000);
    const int qo = 2 * r * HD * 2 + c * 16;
    wr[0] = ldbuf<2>(rq, qo, 0); wr[1] = ldbuf<2>(rq, qo, HD * 2); wr[2] = ldbuf<2>(rq, qo, 32 * HD * 2); wr[3] = ldbuf<2>(rq, qo, 33 * HD * 2);
    wr[4] = ldbuf<2>(rq2, qo, 0); wr[5] = ldbuf<2>(rq2, qo, HD * 2);
  }
  if (tid == 0) *(LAS u32x4*)(L + L_CTR) = u32x4{0u, 0u, 0u, 0u};
  asm volatile("" :: "s"(kb));
  __syncthreads();
  const u32 jit = (u32)(((j * 8 + xg * 33) & 255) * TAU) >> 8;
  const u32 t0 = now16() + jit;

  if (__builtin_expect(w >= 4, 1)) {
    const u16* vb = p.vc + kvoff;
    const u32 land = (u32)(size_t)(L + L_IMG) + sw * 32768;
    __builtin_amdgcn_s_waitcnt(0x0F70);
    P.due = t0;
#pragma nounroll
    for (int g = 0; g < 8; ++g) {
      pace(P, TAU0);
#pragma unroll
      for (int i = 0; i < 4; ++i) dma16_nt(kb + (soff(g * 4) + soff(i)) / 2, voff, land + (g * 4 + i) * 1024);
    }
    constexpr int NRW = 4 / NP;
    const bool red = sw < NRW && pi < 72;
    const int unit = red ? pi * NRW + sw : 0;
    const int ocol = pair_col(unit * NP + lane % NP, lane >> 5);
    const u16 xres = ((const GAS u16*)p.x)[b * DM + ocol];
    u32x4 ring[W];
    {
      const __amdgpu_buffer_rsrc_t rk = __builtin_amdgcn_make_buffer_rsrc((void*)(kb + ITS), 0, ITB, 0x00020000);
#pragma unroll
      for (int i = 0; i < W; ++i) { if (i % 4 == 0) pace(P, TAU0); ring[i] = ldbuf<2>(rk, voff, soff(i)); SB(); }
    }
    SB();
    P.due += US(GAP_G);
    lds_wait(L, 1, 1);
    const u32x2 epv = *(LAS u32x2*)(L + L_TAG);
    const u64 ep = ((u64)__builtin_amdgcn_readfirstlane(epv[1]) << 32) | (u32)__builtin_amdgcn_readfirstlane(epv[0]);
    const u32x4 q = *(LAS u32x4*)(L + L_QRED + c * 16);
    float sc[8];
    const float SCALE = 0.08838834764831845f;
    auto qk = [&](const u32x4& kk) { float d = dot2(kk[0], q[0], 0.f); d = dot2(kk[1], q[1], d); d = dot2(kk[2], q[2], d); return dot2(kk[3], q[3], d); };
    {
      asm volatile("s_waitcnt vmcnt(32)" ::: "memory");
      const __amdgpu_buffer_rsrc_t rn = __builtin_amdgcn_make_buffer_rsrc((void*)(kb + 2 * ITS), 0, ITB, 0x00020000);
      LAS u32x4* lz = (LAS u32x4*)(L + L_IMG + sw * 32768) + lane;
      float keep[4] = {0.f, 0.f, 0.f, 0.f};
      u32x4 lk = lz[0];
      SFor<0, W>::run([&](auto tc) {
        constexpr int J = decltype(tc)::value;
        float d = qk(ring[J]);
        asm volatile("" : "+v"(d));
        if constexpr (J % 4 == 0) pace(P, TAU);
        SB();
        ring[J] = ldbuf<2>(rn, voff, soff(J));
        float e = qk(lk);
        asm volatile("" : "+v"(e));
        if constexpr (J + 1 < W) lk = lz[(J + 1) * 64];
        d = row_sum16(d); e = row_sum16(e);
        keep[2 + J / 16] = (c == (J & 15)) ? d : keep[2 + J / 16];
        keep[J / 16] = (c == (J & 15)) ? e : keep[J / 16];
        asm volatile("" : "+v"(keep[J / 16]), "+v"(keep[2 + J / 16]));
        SB();
      });
      lds_arrive(L, 2, lane);
#pragma unroll
      for (int k = 0; k < 4; ++k) { sc[k] = rbf(rbf(keep[k]) * SCALE); sc[4 + k] = 0.f; }
    }
#pragma nounroll
    for (int it = 0; it < 2; ++it) {
      const __amdgpu_buffer_rsrc_t rn = __builtin_amdgcn_make_buffer_rsrc((void*)(it ? vb : kb + 3 * ITS), 0, ITB, 0x00020000);
      float keep0 = 0.f, keep1 = 0.f;
      P.due += it ? US(GAP_O) : US(GAP_G);
      SFor<0, W>::run([&](auto tc) {
        constexpr int J = decltype(tc)::value;
        float d = qk(ring[J]);
        asm volatile("" : "+v"(d));
        if constexpr (J % 4 == 0) pace(P, TAU);
        SB();
        ring[J] = ldbuf<2>(rn, voff, soff(J));
        d = row_sum16(d);
        if constexpr (J < 16) { keep0 = (c == J) ? d : keep0; asm volatile("" : "+v"(keep0)); } else { keep1 = (c == J - 16) ? d : keep1; asm volatile("" : "+v"(keep1)); }
        SB();
      });
      sc[4] = sc[6]; sc[5] = sc[7];
      sc[6] = rbf(rbf(keep0) * SCALE); sc[7] = rbf(rbf(keep1) * SCALE);
    }
    const float mw = wave_max(fmaxf(fmaxf(fmaxf(sc[0], sc[1]), fmaxf(sc[2], sc[3])), fmaxf(fmaxf(sc[4], sc[5]), fmaxf(sc[6], sc[7]))));
#pragma unroll
    for (int k = 0; k < 8; ++k) sc[k] = __builtin_amdgcn_exp2f((sc[k] - mw) * LOG2E);
    {
      const float lw = wave_sum(((sc[0] + sc[1]) + (sc[2] + sc[3])) + ((sc[4] + sc[5]) + (sc[6] + sc[7])));
      if (lane < 2) ((LAS float*)(L + L_ML))[lane * 4 + sw] = lane ? lw : mw;
      lds_arrive(L, 3, lane);
    }
    SB();
    float acc[8] = {0, 0, 0, 0, 0, 0, 0, 0};
    SFor<0, VOFF>::run([&](auto tc) {
      constexpr int J = decltype(tc)::value;
      fma8(acc, dpp<BCAST + (J & 15)>(sc[J / 16]), ring[J]);
      pin8(acc);
      SB();
    });
    P.due += US(GAP_D);
#pragma nounroll
    for (int it = 0; it < 3; ++it) {
      const __amdgpu_buffer_rsrc_t rn = __builtin_amdgcn_make_buffer_rsrc((void*)(vb + (it + 1) * ITS), 0, ITB, 0x00020000);
      SFor<0, W>::run([&](auto tc) {
        constexpr int J = decltype(tc)::value, T = J + VOFF;
        if constexpr (J % 4 == 0) pace(P, TAU);
        SB();
        ring[J] = ldbuf<2>(rn, voff, soff(J));
        SB();
        fma8(acc, dpp<BCAST + (T & 15)>(sc[T / 16]), ring[T % W]);
        pin8(acc);
        SB();
      });
#pragma unroll
      for (int k = 0; k < 6; ++k) sc[k] = sc[k + 2];
    }
    SFor<VOFF, W>::run([&](auto tc) {
      constexpr int J = decltype(tc)::value;
      fma8(acc, dpp<BCAST + (J & 15)>(sc[J / 16]), ring[J]);
      pin8(acc);
      SB();
    });
    {
      float o0, o1;
      rows_reduce_scatter(acc, o0, o1);
      const u32 hi = pkbf(o0, o1);
      const u32 lo = pkbf(o0 - lo16(hi), o1 - hi16(hi));
      LAS u32* ar = (LAS u32*)(L + L_AIMG + (2 * sw) * 272 + (c * 8 + 4 * (r >> 1) + 2 * (r & 1)) * 2);
      ar[0] = hi; ar[68] = lo;
    }
    __syncthreads();
    if (red) {
      wait_until(now16() + US(RDELAY) + (TAU - jit));
      const __amdgpu_buffer_rsrc_t ry = __builtin_amdgcn_make_buffer_rsrc((void*)(p.ws + WS_Y + (size_t)b * (72 * 1024) + (unit * NP / NL) * (256 * NL) + (unit * NP % NL) * 2), 0, 2048 * NL, 0x00020000);
      const int yoff = (lane / NP) * (16 * NL) + (lane % NP) * 16;
      const u32 t8 = (u32)(ep >> 32) & 0xffu;
      u32x4 v[2 * NP];
      bool bad2 = false;
#pragma nounroll
      for (unsigned spins = 0;; ++spins) {
#pragma unroll
        for (int k = 0; k < 2 * NP; ++k) v[k] = __builtin_amdgcn_raw_buffer_load_b128(ry, yoff, k * (64 / NP) * (16 * NL), 16);
        u32 mis = 0;
#pragma unroll
        for (int k = 0; k < 2 * NP; ++k) mis |= (v[k][1] ^ (u32)ep) | (v[k][3] ^ (u32)ep) | (((v[k][0] ^ t8) | (v[k][2] ^ t8)) & 0xffu);
        if (__all(mis == 0)) break;
        if (spins >= SPIN_CAP) { bad2 = true; break; }
        __builtin_amdgcn_s_sleep(1);
        asm volatile("" ::: "memory");
      }
      float s0 = 0.f, s1 = 0.f;
#pragma unroll
      for (int k = 0; k < 2 * NP; ++k) { s0 += __uint_as_float(v[k][0] & 0xffffff00u); s1 += __uint_as_float(v[k][2] & 0xffffff00u); }
      float t = sum32(s0, s1);
      t = sum16(t, t);
      t += dpp<XOR8>(t);
      t += dpp<0x124>(t);
      if (NP <= 2) t += dpp<XOR2>(t);
      if (NP <= 1) t += dpp<XOR1>(t);
      float res = __builtin_bit_cast(float, (u32)xres << 16) + t;
      if (bad2) res = __builtin_nanf("");
      if ((lane & 31) < NP) ((GAS u16*)p.out)[b * DM + ocol] = (u16)(pkbf(res, res) & 0xffffu);
      if (sw == 0 && pi == 0 && b == 0 && lane == 0) __hip_atomic_store(ws + WS_EPOCH, ep, RLX_AGENT);
    }
  } else {
    const int a = w, i8 = j & 7;
    const u64 ep_old = *(const __attribute__((address_space(4))) u64*)(p.ws + WS_EPOCH);
    const __amdgpu_buffer_rsrc_t rws = __builtin_amdgcn_make_buffer_rsrc((void*)p.ws, 0, WS_END * 8, 0x00020000);
    const GAS u32x4* xp = (const GAS u32x4*)(p.x + (lane >> 5) * DM);
    const u32x4 xa = xp[lane & 31], xb = xp[32 + (lane & 31)], xe = xp[64 + (lane & 7)];
    const GAS u32* x32 = (const GAS u32*)p.x;
    const int st3 = (c >> 1) < 2 ? a + 4 * (c >> 1) : 8;
    const u32 xq0 = x32[(c & 1) * (DM / 2) + 36 * i8 + 4 * st3 + r];
    const bool fprod = j < 27;
    const int fidx = fprod ? xg * 27 + j : 0, fdc = fidx / 6, fks = fidx % 6;
    const int frow = 96 * fks + 48 * (a & 1);
    const u32 xf0 = x32[(c & 1) * (DM / 2) + frow / 2 + 4 * ((c >> 1) < 6 ? (c >> 1) : 0) + r];
    SB();
    u32 PQ, PF;
    {
      float v[24];
#pragma unroll
      for (int k = 0; k < 4; ++k) {
        v[2 * k] = lo16(xa[k]); v[2 * k + 1] = hi16(xa[k]); v[8 + 2 * k] = lo16(xb[k]); v[8 + 2 * k + 1] = hi16(xb[k]);
        v[16 + 2 * k] = lo16(xe[k]); v[16 + 2 * k + 1] = hi16(xe[k]);
      }
      float s0 = 0.f, s1 = 0.f, s2 = 0.f;
#pragma unroll
      for (int k = 0; k < 8; ++k) { s0 += v[k]; s1 += v[8 + k]; s2 += v[16 + k]; }
      const bool t8 = (lane & 31) < 8;
      float mean = row_sum16(s0 + s1 + (t8 ? s2 : 0.f)); mean = sum16(mean, mean) * (1.0f / DM);
      float q0 = 0.f, q1 = 0.f, q2 = 0.f;
#pragma unroll
      for (int k = 0; k < 8; ++k) { float d0 = v[k] - mean, d1 = v[8 + k] - mean, d2 = v[16 + k] - mean; q0 = fmaf(d0, d0, q0); q1 = fmaf(d1, d1, q1); q2 = fmaf(d2, d2, q2); }
      float var = row_sum16(q0 + q1 + (t8 ? q2 : 0.f)); var = sum16(var, var) * (1.0f / DM);
      const float mbv = rbf(mean), denv = rbf(sqrtf(rbf(rbf(var) + 1e-5f)));
      const float mb0 = rdlane(mbv, 0), mb1 = rdlane(mbv, 32), dn0 = rdlane(denv, 0), dn1 = rdlane(denv, 32);
      const float mq = (c & 1) ? mb1 : mb0, dq = (c & 1) ? dn1 : dn0;
      PQ = pkbf(rbf(lo16(xq0) - mq) / dq, rbf(hi16(xq0) - mq) / dq);
      PF = pkbf(rbf(lo16(xf0) - mq) / dq, rbf(hi16(xf0) - mq) / dq);
    }
    const u64 ep = next_epoch(ep_old);
    {
      float qa[8] = {0, 0, 0, 0, 0, 0, 0, 0}, qb[8] = {0, 0, 0, 0, 0, 0, 0, 0};
      SFor<0, 3>::run([&](auto mc) {
        constexpr int m = decltype(mc)::value;
        const u32 x0 = dppu<BCAST + 2 * m>(PQ), x1 = dppu<BCAST + 2 * m + 1>(PQ);
#pragma unroll
        for (int k = 0; k < 4; ++k) {
          const u32 pl = __builtin_amdgcn_perm(wr[2 * m + 1][k], wr[2 * m][k], 0x05040100u), ph = __builtin_amdgcn_perm(wr[2 * m + 1][k], wr[2 * m][k], 0x07060302u);
          qa[2 * k] = dot2(pl, x0, qa[2 * k]); qa[2 * k + 1] = dot2(ph, x0, qa[2 * k + 1]);
          qb[2 * k] = dot2(pl, x1, qb[2 * k]); qb[2 * k + 1] = dot2(ph, x1, qb[2 * k + 1]);
        }
      });
      float o0, o1, o2, o3;
      rows_reduce_scatter(qa, o0, o1);
      rows_reduce_scatter(qb, o2, o3);
      LAS float* qp = (LAS float*)(L + L_QP) + a * 256 + c * 8 + 4 * (r >> 1) + 2 * (r & 1);
      *(LAS u32x2*)qp = u32x2{__float_as_uint(o0), __float_as_uint(o1)};
      *(LAS u32x2*)(qp + 128) = u32x2{__float_as_uint(o2), __float_as_uint(o3)};
      if (tid == 0) *(LAS u64*)(L + L_TAG) = ep;
      aux_sync(L, 4, lane);
      if (a == 1) {
        LAS f32x4* qs = (LAS f32x4*)(L + L_QP) + lane;
        const f32x4 part = (qs[0] + qs[64]) + (qs[128] + qs[192]);
        __builtin_amdgcn_raw_buffer_store_b128(__builtin_bit_cast(u32x4, part), rws, WS_Q * 8 + (h * 8 + i8) * 1024 + lane * 16, 0, 16);
        asm volatile("s_waitcnt vmcnt(0)" ::: "memory");
        if (lane == 0) __hip_atomic_store(ws + WS_QF + (h * 8 + i8) * 16, ep, RLX_AGENT);
      }
    }
    {
      typedef const __attribute__((address_space(4))) u32* cp;
      const u16* vq = p.vc + (size_t)bh * SK * DH;
      const u16* pa = a == 0 ? vq : a == 1 ? p.wo + (size_t)h * DH * DM : a == 2 ? p.wg + (size_t)frow * FF : p.wu + (size_t)frow * FF;
      const u16* pb = a == 0 ? vq + SK * DH - 64 : a == 1 ? p.wd + (size_t)pi * 36 * DM : a == 2 ? p.kc + (size_t)bh * SK * DH + SK * DH - 64 : p.out;
      const u32 z0 = *(cp)pa, z1 = *(cp)pb;
      asm volatile("" :: "s"(z0), "s"(z1));
    }
    float wa, wb;
    {
      const int rr = lane >> 3, ch = lane & 7;
      const __amdgpu_buffer_rsrc_t rg = __builtin_amdgcn_make_buffer_rsrc((void*)((a & 2 ? p.wu : p.wg) + (size_t)frow * FF + fdc * 128), 0, fprod ? 47 * FF * 2 + 256 : 0, 0x00020000);
      const __amdgpu_buffer_rsrc_t rd = __builtin_amdgcn_make_buffer_rsrc((void*)(p.wd + (size_t)(pi * 36 + 9 * a) * DM), 0, 9 * DM * 2, 0x00020000);
      const int fo = 2 * r * FF * 2 + c * 16;
      const u16* wsrc = p.wo + (size_t)(h * DH + 32 * a) * DM;
      const u32 wdst = (u32)(size_t)(L + L_IMG) + a * 36864;
      const int u0 = pi * 36 + 9 * a;
      bool bad = false;
      if (a == 1) {
        wait_until(t0 + US(TQG));
#pragma nounroll
        for (unsigned spins = 0;; ++spins) {
          if (__all(ld_gran(ws + WS_QF + (h * 8 + (lane & 7)) * 16) == ep)) break;
          if (spins >= SPIN_CAP) { bad = true; break; }
          __builtin_amdgcn_s_sleep(8);
        }
        asm volatile("" ::: "memory");
        f32x4 v[4];
#pragma unroll
        for (int k = 0; k < 4; ++k)
          v[k] = __builtin_bit_cast(f32x4, __builtin_amdgcn_raw_buffer_load_b128(rws, WS_Q * 8 + (h * 8 + 2 * k + (lane >> 5)) * 1024 + b * 512 + (lane & 31) * 16, 0, 16));
        const f32x4 t = (v[0] + v[1]) + (v[2] + v[3]);
        const float q0 = sum32(t[0], t[2]), q1 = sum32(t[1], t[3]);
        ((LAS u32*)(L + L_QRED))[2 * (lane & 31) + (lane >> 5)] = pkbf(q0, q1);
        lds_arrive(L, 1, lane);
      }
      {
        u32x4 fr[12];
        wait_until(t0 + T_B2);
#pragma unroll
        for (int i = 0; i < 6; ++i) fr[i] = ldbuf<2>(rg, fo, (8 * (i / 2) + (i & 1)) * FF * 2);
        SB();
        wait_until(t0 + T_B3);
#pragma unroll
        for (int i = 6; i < 12; ++i) fr[i] = ldbuf<2>(rg, fo, (8 * (i / 2) + (i & 1)) * FF * 2);
        SB();
        float fa[8] = {0, 0, 0, 0, 0, 0, 0, 0}, fb[8] = {0, 0, 0, 0, 0, 0, 0, 0};
        SFor<0, 6>::run([&](auto mc) {
          constexpr int m = decltype(mc)::value;
          const u32 x0 = dppu<BCAST + 2 * m>(PF), x1 = dppu<BCAST + 2 * m + 1>(PF);
#pragma unroll
          for (int k = 0; k < 4; ++k) {
            const u32 pl = __builtin_amdgcn_perm(fr[2 * m + 1][k], fr[2 * m][k], 0x05040100u), ph = __builtin_amdgcn_perm(fr[2 * m + 1][k], fr[2 * m][k], 0x07060302u);
            fa[2 * k] = dot2(pl, x0, fa[2 * k]); fa[2 * k + 1] = dot2(ph, x0, fa[2 * k + 1]);
            fb[2 * k] = dot2(pl, x1, fb[2 * k]); fb[2 * k + 1] = dot2(ph, x1, fb[2 * k + 1]);
          }
        });
        f32x4 o;
        { float o0, o1, o2, o3; rows_reduce_scatter(fa, o0, o1); rows_reduce_scatter(fb, o2, o3); o = f32x4{o0, o1, o2, o3}; }
        LAS f32x4* fx = (LAS f32x4*)(L + L_FRED) + (a >> 1) * 64 + lane;
        if (a & 1) *fx = o;
        aux_sync(L, 8, lane);
        if (!(a & 1) && fprod) {
          o += *fx;
          __builtin_amdgcn_raw_buffer_store_b128(__builtin_bit_cast(u32x4, o), rws, WS_H * 8 + (((a >> 1) * 6 + fks) * 2304 + fdc * 64 + c * 4 + 2 * (r >> 1) + (r & 1)) * 16, 0, 16);
          asm volatile("s_waitcnt vmcnt(0)" ::: "memory");
          if (lane == 0) __hip_atomic_store(ws + WS_HF + (((a >> 1) * 6 + fks) * 36 + fdc) * 16, ep, RLX_AGENT);
        }
      }
      lds_wait(L, 2, 4);
      int wp = (i8 * 9) >> 1;
      auto wo_piece = [&]() { dma16(wsrc + wp * 512, lane * 16, wdst + wp * 1024); wp = wp == 35 ? 0 : wp + 1; };
      auto wo_trickle = [&](int n) {
#pragma nounroll
        for (int i = 0; i < n; ++i) { P.due += wait_until(P.due) + US(TWO); wo_piece(); }
      };
      wait_until(t0 + T_B4);
#pragma nounroll
      for (int i = 0; i < 5; ++i) wo_piece();
      const int hdc = xg * 5 + (j - 27);
      if (!fprod && hdc < 36 && a == 0) {
        wait_until(t0 + T_B4 + US(THG));
#pragma nounroll
        for (unsigned spins = 0;; ++spins) {
          if (__all(ld_gran(ws + WS_HF + ((lane < 12 ? lane : 0) * 36 + hdc) * 16) == ep)) break;
          if (spins >= SPIN_CAP) { bad = true; break; }
          __builtin_amdgcn_s_sleep(8);
        }
        asm volatile("" ::: "memory");
        f32x4 v[12];
#pragma unroll
        for (int k = 0; k < 12; ++k) v[k] = __builtin_bit_cast(f32x4, __builtin_amdgcn_raw_buffer_load_b128(rws, WS_H * 8 + (hdc * 64 + lane) * 16, k * 2304 * 16, 16));
        const f32x4 g4 = ((v[0] + v[1]) + (v[2] + v[3])) + (v[4] + v[5]), u4 = ((v[6] + v[7]) + (v[8] + v[9])) + (v[10] + v[11]);
        float hv[4];
#pragma unroll
        for (int e = 0; e < 4; ++e) {
          const float gs = rbf(g4[e]), us = rbf(u4[e]);
          const float inner = 0.7978845608028654f * (gs + 0.044715f * gs * gs * gs);
          const float ge = rbf(gs / (1.0f + __expf(-2.0f * inner)));
          hv[e] = bad ? __builtin_nanf("") : ge * us;
        }
        __hip_atomic_store(ws + WS_HG + hdc * 64 + lane, (ep << 32) | pkbf(hv[0], hv[1]), RLX_AGENT);
        __hip_atomic_store(ws + WS_HG + 2304 + hdc * 64 + lane, (ep << 32) | pkbf(hv[2], hv[3]), RLX_AGENT);
      }
      P.due = t0 + T_B4 + US(1.5) + a * (US(TWO) / 4);
      wo_trickle(8);
      u32x4 dw[11];
      wait_until(t0 + T_B5);
#pragma unroll
      for (int i = 0; i < 9; ++i) dw[i] = ldbuf<0>(rd, lane * 16, i * DM * 2);
      dw[9] = ldbuf<0>(rd, rr * DM * 2 + 1024 + ch * 16, 0);
      dw[10] = ldbuf<0>(rd, 8 * DM * 2 + 1024 + ch * 16, 0);
      const int hu = u0 + (ch == 7 ? 8 : rr);
      GAS u64* hgp = ws + WS_HG + b * 2304 + (hu >> 1);
      u64 g64 = ld_gran(hgp);
      SB();
      P.due = t0 + T_B5 + US(1.0) + a * (US(TWO) / 4);
      wo_trickle(3);
      if (a == 3) {
        lds_wait(L, 3, 4);
        if (lane < 8) st_gran<8>(ws + WS_STAT + bh * 64 + s * 16 + lane, ep, ((LAS float*)(L + L_ML))[lane]);
      }
#pragma nounroll
      for (unsigned spins = 0; !__all((u32)(g64 >> 32) == (u32)ep); ++spins) {
        if (spins >= SPIN_CAP) { bad = true; break; }
        __builtin_amdgcn_s_sleep(8);
        g64 = ld_gran(hgp);
      }
      {
        const float hx = (hu & 1) ? hi16((u32)g64) : lo16((u32)g64);
        float hh[2];
        hh[0] = dpp<0x00>(hx);
        hh[1] = rdlane(hx, 7);
        float ym[8] = {0, 0, 0, 0, 0, 0, 0, 0}, yt[8] = {0, 0, 0, 0, 0, 0, 0, 0};
#pragma unroll
        for (int i = 0; i < 8; ++i) fma8(ym, rdlane(hh[0], 8 * i), dw[i]);
        fma8(ym, hh[1], dw[8]);
        fma8(yt, hh[0], dw[9]);
        fma8(yt, lane < 8 ? hh[1] : 0.f, dw[10]);
#pragma unroll
        for (int k = 0; k < 8; ++k) { float t = yt[k]; t += dpp<XOR8>(t); yt[k] = xrow_sum(t); }
        LAS float* yf = (LAS float*)(L + L_YF) + a * 576;
        *(LAS f32x4*)(yf + lane * 8) = f32x4{ym[0], ym[1], ym[2], ym[3]};
        *(LAS f32x4*)(yf + lane * 8 + 4) = f32x4{ym[4], ym[5], ym[6], ym[7]};
        if (lane < 8) { *(LAS f32x4*)(yf + 512 + lane * 8) = f32x4{yt[0], yt[1], yt[2], yt[3]}; *(LAS f32x4*)(yf + 512 + lane * 8 + 4) = f32x4{yt[4], yt[5], yt[6], yt[7]}; }
      }
      SB();
      wo_trickle(10);
      GAS u64* sgp = ws + WS_STAT + bh * 64 + (c >> 2) * 16 + (r & 1) * 4 + (c & 3);
      g64 = ld_gran(sgp);
      wo_trickle(10);
#pragma nounroll
      for (unsigned spins = 0; !__all(gran_ok<8>(g64, ep)); ++spins) {
        if (spins >= SPIN_CAP) { bad = true; break; }
        __builtin_amdgcn_s_sleep(8);
        g64 = ld_gran(sgp);
      }
      const u32 sv = __float_as_uint(gran_val<8>(g64));
      auto t = __builtin_amdgcn_permlane16_swap(sv, sv, false, false);
      const float mi = __uint_as_float(t[0]), li = __uint_as_float(t[1]);
      const float M = row_max16(mi);
      const float ei = __builtin_amdgcn_exp2f((mi - M) * LOG2E);
      float fi = ei * __builtin_amdgcn_rcpf(row_sum16(li * ei));
      if (bad) fi = __builtin_nanf("");
      const float f0 = rdlane(fi, 4 * s), f1 = rdlane(fi, 4 * s + 1), f2 = rdlane(fi, 4 * s + 2), f3 = rdlane(fi, 4 * s + 3);
      wa = (r & 1) ? f2 : f0; wb = (r & 1) ? f3 : f1;
      asm volatile("" : "+v"(wa), "+v"(wb));
    }
    asm volatile("s_waitcnt vmcnt(0)" ::: "memory");
    aux_sync(L, 12, lane);
    float yfs[3];
    {
      LAS float* yf = (LAS float*)(L + L_YF) + 144 * a;
#pragma unroll
      for (int t = 0; t < 3; ++t) { const int o = t < 2 ? 64 * t + lane : 128 + c; yfs[t] = (yf[o] + yf[576 + o]) + (yf[1152 + o] + yf[1728 + o]); }
      asm volatile("" : "+v"(yfs[0]), "+v"(yfs[1]), "+v"(yfs[2]));
    }
    u32x2 bf[9][4][2];
    {
      LAS unsigned char* tb = L + L_IMG + (8 * r + (c >> 2)) * 1152 + (144 * a + 4 * (c & 3)) * 2;
#pragma unroll
      for (int t = 0; t < 9; ++t)
#pragma unroll
        for (int ks = 0; ks < 4; ++ks)
#pragma unroll
          for (int u = 0; u < 2; ++u)
            bf[t][ks][u] = __builtin_bit_cast(u32x2, __builtin_amdgcn_ds_read_tr16_b64_v4i16((LAS s16x4*)(tb + (32 * ks + 4 * u) * 1152 + t * 32)));
#pragma unroll
      for (int t = 0; t < 9; ++t)
#pragma unroll
        for (int ks = 0; ks < 4; ++ks) asm volatile("" : "+v"(bf[t][ks][0]), "+v"(bf[t][ks][1]));
    }
    const int P0 = 72 * a + lane, P1 = 72 * a + 64 + (lane & 7);
    int o0 = WS_Y * 8 + b * (72 * 8192) + (((P0 / NL) * 128 + pi) * NL + P0 % NL) * 16, o1 = WS_Y * 8 + b * (72 * 8192) + (((P1 / NL) * 128 + pi) * NL + P1 % NL) * 16;
    if (lane >= 8) o1 = 0x7ffffff0;
    asm volatile("" : "+v"(o0), "+v"(o1));
    SB();
    __syncthreads();
    {
      u32x4 af[4];
#pragma unroll
      for (int ks = 0; ks < 4; ++ks) af[ks] = *(LAS u32x4*)(L + L_AIMG + (lane & 7) * 272 + ks * 64 + r * 16);
      SB();
      f32x4 d[9];
      auto mm = [&](int t, int ks) {
        const u32x4 bb = {bf[t][ks][0][0], bf[t][ks][0][1], bf[t][ks][1][0], bf[t][ks][1][1]};
        d[t] = __builtin_amdgcn_mfma_f32_16x16x32_bf16(__builtin_bit_cast(bf16x8, af[ks]), __builtin_bit_cast(bf16x8, bb), d[t], 0, 0, 0);
      };
      auto fin = [&](int t) { return fmaf(wa, d[t][0] + d[t][1], wb * (d[t][2] + d[t][3])); };
#pragma unroll
      for (int t = 0; t < 9; ++t) d[t] = f32x4{0.f, 0.f, 0.f, 0.f};
      auto mm4 = [&](int t) { mm(t, 0); mm(t, 1); mm(t, 2); mm(t, 3); };
      const bool lo32 = lane < 32;
      mm(8, 0); mm(0, 0); mm(8, 1); mm(0, 1); mm(8, 2); mm(0, 2); mm(8, 3); mm(0, 3); SB();
      mm4(1); float e8 = fin(8); e8 = sum16(e8, e8) + yfs[2]; SB();
      mm4(2); const float e0 = fin(0);
      __builtin_amdgcn_raw_buffer_store_b128(u32x4{gran_lo(ep, e8), (u32)ep, gran_lo(ep, dpp<XOR8>(e8)), (u32)ep}, rws, o1, 0, 16);
      SB();
      mm4(3); const float s01 = sum16(e0, fin(1)); SB();
      mm4(4); const float e2 = fin(2); SB();
      mm4(5); const float s23 = sum16(e2, fin(3)); const float y0 = (lo32 ? s01 : s23) + yfs[0]; SB();
      mm4(6); const float e4 = fin(4); SB();
      mm4(7); const float s45 = sum16(e4, fin(5)); SB();
      const float s67 = sum16(fin(6), fin(7));
      const float y1 = (lo32 ? s45 : s67) + yfs[1];
      __builtin_amdgcn_raw_buffer_store_b128(u32x4{gran_lo(ep, y0), (u32)ep, gran_lo(ep, y1), (u32)ep}, rws, o0, 0, 16);
    }
  }
}

extern "C" void kernel_launch(void* const* d_in, const int* in_sizes, int n_in, void* d_out, int out_size, void* d_ws, size_t ws_size, hipStream_t stream) {
  Params p;
  p.x = (const u16*)d_in[0]; p.kc = (const u16*)d_in[1]; p.vc = (const u16*)d_in[2]; p.wq = (const u16*)d_in[3]; p.wo = (const u16*)d_in[4];
  p.wg = (const u16*)d_in[6]; p.wu = (const u16*)d_in[7]; p.wd = (const u16*)d_in[8];
  p.out = (u16*)d_out; p.ws = (u64*)d_ws;
  fused_decode<<<256, NTHR, 0, stream>>>(p);
}
```
